```python
import torch

def _softmax(x, axis=-1):
    return torch.softmax(x, dim=axis)
def _gelu(x):
    return torch.nn.functional.gelu(x, approximate="tanh")
def _silu(x):
    return torch.nn.functional.silu(x)
def _sigmoid(x):
    return torch.sigmoid(x)
def _layernorm(x, axis=-1, eps=1e-5):
    m = x.mean(dim=axis, keepdim=True)
    v = x.var(dim=axis, keepdim=True, unbiased=False)
    return (x - m) / torch.sqrt(v + eps)
def _scatter_add(tgt, idx, src, axis=0):
    return tgt.clone().index_add_(axis, idx, src)
def _segment_sum(src, seg, n):
    out = torch.zeros((n,) + src.shape[1:], dtype=src.dtype)
    return out.index_add_(0, seg, src)
def _pad(x, pw):
    flat = tuple(v for pair in reversed(pw) for v in pair)
    return torch.nn.functional.pad(x, flat)

kernel_name = 'gated_mix__linear_attn_block__linear_kernel_hedgehog__prompt'

def setup_inputs():

    _w = (lambda a: (lambda t: t.to(torch.bfloat16) if t.is_floating_point() else t)(torch.as_tensor(a)))
    def _A_setup():
        B, S, D = 1, 2048, 576
        return {
            "x": _w((lambda *s: torch.randn(*s, dtype=torch.bfloat16) * 0.05)(B, S, D)),
            "wqkv": _w((lambda *s: torch.randn(*s, dtype=torch.bfloat16) * 1.6)(D, 3 * D)),
            "wo": _w((lambda *s: torch.randn(*s, dtype=torch.bfloat16) * 0.1)(D, D)),
        }
    _A_in = _A_setup()

    def _B_setup():
        B, S, D, H = 1, 2048, 576, 32
        Dh = D // H
        return {
            "x": _w((lambda *s: torch.randn(*s, dtype=torch.bfloat16) * 0.05)(B, S, D)),
            "wqkv": _w((lambda *s: torch.randn(*s, dtype=torch.bfloat16) * 1.6)(D, 3 * D)),
            "w_phi": _w((lambda *s: torch.randn(*s, dtype=torch.bfloat16) * 1.6)(H, Dh, Dh) * (Dh ** -0.5)),
            "wo": _w((lambda *s: torch.randn(*s, dtype=torch.bfloat16) * 0.05)(D, D)),
        }
    _B_in = _B_setup()

    return {
        "A_x": _A_in["x"],
        "A_wqkv": _A_in["wqkv"],
        "A_wo": _A_in["wo"],
        "B_x": _B_in["x"],
        "B_wqkv": _B_in["wqkv"],
        "B_w_phi": _B_in["w_phi"],
        "B_wo": _B_in["wo"],
        "gate_w": _w((lambda *s: torch.randn(*s, dtype=torch.bfloat16) * 0.05)(576) * 16.0),
    }

def reference(A_x, A_wqkv, A_wo, B_x, B_wqkv, B_w_phi, B_wo, gate_w):
    def _A_ref(x, wqkv, wo):
        B, S, D = x.shape
        H = 32; Dh = D // H
        qkv = (lambda x, *d: x.permute(*d))((x @ wqkv).reshape(B, S, 3, H, Dh), 2, 0, 3, 1, 4)
        q, k, v = qkv[0], qkv[1], qkv[2]
        phi = lambda t: torch.where(t > 0, t + 1, torch.exp(t)) + 1e-6
        pq, pk = phi(q), phi(k)
        kv = torch.einsum("bhsd,bhse->bhde", pk, v)
        z = torch.einsum("bhsd->bhd", pk)
        num = torch.einsum("bhqd,bhde->bhqe", pq, kv)
        den = torch.einsum("bhqd,bhd->bhq", pq, z)[..., None] + 1e-6
        o = num / den
        return (lambda x, *d: x.permute(*d))(o, 0, 2, 1, 3).reshape(B, S, D) @ wo

    def _B_ref(x, wqkv, w_phi, wo):
        B, S, D = x.shape
        H = 32; Dh = D // H
        qkv = (lambda x, *d: x.permute(*d))((x @ wqkv).reshape(B, S, 3, H, Dh), 2, 0, 3, 1, 4)
        q, k, v = qkv[0], qkv[1], qkv[2]
        pq = torch.einsum("bhsd,hdf->bhsf", q, w_phi)
        pk = torch.einsum("bhsd,hdf->bhsf", k, w_phi)
        phi_q = torch.concatenate([_softmax(pq, axis=-1), _softmax(-pq, axis=-1)], axis=-1)
        phi_k = torch.concatenate([_softmax(pk, axis=-1), _softmax(-pk, axis=-1)], axis=-1)
        kv = torch.einsum("bhsf,bhsd->bhfd", phi_k, v)
        z  = torch.einsum("bhsf->bhf", phi_k)
        num = torch.einsum("bhqf,bhfd->bhqd", phi_q, kv)
        den = torch.einsum("bhqf,bhf->bhq", phi_q, z)[..., None] + 1e-6
        o = num / den
        return (lambda x, *d: x.permute(*d))(o, 0, 2, 1, 3).reshape(B, S, D) @ wo

    x = A_x
    g = _sigmoid((x * gate_w).sum(-1, keepdims=True))
    a = _A_ref(x, A_wqkv, A_wo)
    b = _B_ref(x, B_wqkv, B_w_phi, B_wo)
    return g * a + (1 - g) * b
```

```cpp
#include <hip/hip_runtime.h>
#include <hip/hip_bf16.h>
#include <stdint.h>

typedef unsigned short u16;
typedef unsigned int u32;
typedef __attribute__((ext_vector_type(8))) short bf16x8;
typedef __attribute__((ext_vector_type(4))) short s16x4;
typedef __attribute__((ext_vector_type(4))) float f32x4;
typedef __attribute__((ext_vector_type(4))) unsigned u32x4;
typedef __attribute__((ext_vector_type(2))) unsigned u32x2;
typedef __attribute__((ext_vector_type(2))) __bf16 bf16x2n;
typedef __attribute__((address_space(3))) const char* lds_cptr;
typedef __attribute__((address_space(3))) char* lds_ptr;
typedef short v4i16_t __attribute__((ext_vector_type(4)));
#define AS3 __attribute__((address_space(3)))
#define DEV __device__ __forceinline__

constexpr int SEQ = 2048, DM = 576, NH = 32, DH = 18;
constexpr int QP = 1152;
constexpr int WP = 1728;
constexpr int PARTP = 1028;
constexpr int KVA_OFF = 0, ZA_OFF = 324, KVB_OFF = 344, ZB_OFF = 992;

DEV float bf2f(u16 h) { return __uint_as_float(((u32)h) << 16); }
DEV float rbf(float f) { return (float)(__bf16)f; }
DEV u32 pack2(float lo, float hi) { bf16x2n v = {(__bf16)lo, (__bf16)hi}; return __builtin_bit_cast(u32, v); }
DEV float lo2f(u32 p) { return __uint_as_float(p << 16); }
DEV float hi2f(u32 p) { return __uint_as_float(p & 0xffff0000u); }
DEV s16x4 trread(lds_cptr p) { return __builtin_bit_cast(s16x4, __builtin_amdgcn_ds_read_tr16_b64_v4i16((AS3 v4i16_t*)p)); }
DEV bf16x8 cat8(s16x4 lo, s16x4 hi) { return __builtin_shufflevector(lo, hi, 0, 1, 2, 3, 4, 5, 6, 7); }
DEV bf16x8 mk8(u32 a, u32 b, u32 c, u32 d) { u32x4 v = {a, b, c, d}; return __builtin_bit_cast(bf16x8, v); }
#define MFMA16(a, b, c) __builtin_amdgcn_mfma_f32_16x16x32_bf16((a), (b), (c), 0, 0, 0)
#define WAIT_V(n) asm volatile("s_waitcnt vmcnt(" #n ")" ::: "memory")
#define WAIT_L0() asm volatile("s_waitcnt lgkmcnt(0)" ::: "memory")
#define RAW_BAR() __builtin_amdgcn_s_barrier()

DEV float phiA(float t) {
  float u = t > 0.f ? rbf(t + 1.0f) : rbf(__expf(t));
  return rbf(u + 1e-6f);
}

struct HFeat { float fp[4], fm[4], xp[2], xm[2]; };
DEV void hedgehog(bf16x8 raw, bf16x8 a0, bf16x8 a1, int g, HFeat& o) {
  f32x4 z4 = {0.f, 0.f, 0.f, 0.f};
  f32x4 d0 = MFMA16(a0, raw, z4);
  f32x4 d1 = MFMA16(a1, raw, z4);
  float p0 = rbf(d0[0]), p1 = rbf(d0[1]), p2 = rbf(d0[2]), p3 = rbf(d0[3]);
  float e0 = rbf(d1[0]), e1 = rbf(d1[1]);
  float mx = fmaxf(fmaxf(p0, p1), fmaxf(p2, p3));
  float mn = fminf(fminf(p0, p1), fminf(p2, p3));
  if (g == 0) { mx = fmaxf(mx, fmaxf(e0, e1)); mn = fminf(mn, fminf(e0, e1)); }
  mx = fmaxf(mx, __shfl_xor(mx, 16)); mx = fmaxf(mx, __shfl_xor(mx, 32));
  mn = fminf(mn, __shfl_xor(mn, 16)); mn = fminf(mn, __shfl_xor(mn, 32));
  float a[4] = {__expf(p0 - mx), __expf(p1 - mx), __expf(p2 - mx), __expf(p3 - mx)};
  float b[4] = {__expf(mn - p0), __expf(mn - p1), __expf(mn - p2), __expf(mn - p3)};
  float xa0 = __expf(e0 - mx), xa1 = __expf(e1 - mx), xb0 = __expf(mn - e0), xb1 = __expf(mn - e1);
  float sp = (a[0] + a[1]) + (a[2] + a[3]);
  float sm = (b[0] + b[1]) + (b[2] + b[3]);
  if (g == 0) { sp += xa0 + xa1; sm += xb0 + xb1; }
  sp += __shfl_xor(sp, 16); sp += __shfl_xor(sp, 32);
  sm += __shfl_xor(sm, 16); sm += __shfl_xor(sm, 32);
  float ip = 1.0f / sp, im = 1.0f / sm;
#pragma unroll
  for (int i = 0; i < 4; ++i) { o.fp[i] = rbf(a[i] * ip); o.fm[i] = rbf(b[i] * im); }
  o.xp[0] = rbf(xa0 * ip); o.xp[1] = rbf(xa1 * ip);
  o.xm[0] = rbf(xb0 * im); o.xm[1] = rbf(xb1 * im);
}

DEV void load_wphi_frags(const u16* __restrict__ wphi, int head, int lane, bf16x8& a0, bf16x8& a1) {
  const int m = lane & 15, g = lane >> 4;
  u32 w0[4], w1[4];
#pragma unroll
  for (int jj = 0; jj < 4; ++jj) {
    u32 v0 = 0, v1 = 0;
#pragma unroll
    for (int h2 = 0; h2 < 2; ++h2) {
      const int f = 8 * g + 2 * jj + h2;
      u32 e0 = 0, e1 = 0;
      if (f < DH) {
        e0 = wphi[(head * DH + f) * DH + m];
        if (m < 2) e1 = wphi[(head * DH + f) * DH + 16 + m];
      }
      v0 |= e0 << (16 * h2); v1 |= e1 << (16 * h2);
    }
    w0[jj] = v0; w1[jj] = v1;
  }
  a0 = mk8(w0[0], w0[1], w0[2], w0[3]);
  a1 = mk8(w1[0], w1[1], w1[2], w1[3]);
}

constexpr int K1_XB = 16384;
constexpr int K1_WPITCH = 448;
constexpr int K1_STAGE = K1_XB + 64 * K1_WPITCH;
constexpr int EP = 272;
constexpr int E_ROWS = 224;
constexpr int PHI_OFF = E_ROWS * EP;
static_assert(PHI_OFF + 160 * EP <= 3 * K1_STAGE, "k1 lds");

__global__ __launch_bounds__(256, 1)
void k1_qkv(const u16* __restrict__ x, const u16* __restrict__ wA, const u16* __restrict__ wB,
            const u16* __restrict__ wphi, u16* __restrict__ qbuf, float* __restrict__ part) {
  __shared__ __attribute__((aligned(16))) char smem[3 * K1_STAGE];
  const int tid = threadIdx.x, lane = tid & 63;
  const int w = __builtin_amdgcn_readfirstlane(tid >> 6);
  const int b = blockIdx.x;
  const int g4 = b & 7, br = (b >> 3) & 1, rb = b >> 4;
  const u16* __restrict__ W = br ? wB : wA;
  const int wm = w >> 1, wn = w & 1;
  const int fr = lane & 15, fg = lane >> 4;

  const char* xsrc[4];
#pragma unroll
  for (int j = 0; j < 4; ++j) {
    const int rl = (w * 4 + j) * 8 + (lane >> 3);
    const int chunk = (lane & 7) ^ ((rl >> 1) & 7);
    xsrc[j] = (const char*)x + (size_t)(rb * 128 + rl) * (DM * 2) + chunk * 16;
  }
  const char* wsrc[7];
#pragma unroll
  for (int j = 0; j < 7; ++j) {
    const int slot = (w * 7 + j) * 64 + lane;
    const int k = slot / 28, pos = slot % 28;
    int u = pos ^ (((k >> 3) & 1) << 1);
    if (u > 26) u = 26;
    const int seg = u / 9, wi = u % 9;
    wsrc[j] = (const char*)W + (size_t)k * (WP * 2) + (seg * DM + g4 * 72 + wi * 8) * 2;
  }
  auto issue = [&](int st, int buf) {
    char* sb = smem + buf * K1_STAGE;
#pragma unroll
    for (int j = 0; j < 4; ++j)
      __builtin_amdgcn_global_load_lds((const u32*)(xsrc[j] + st * 128), (u32*)(sb + (w * 4 + j) * 1024), 16, 0, 0);
#pragma unroll
    for (int j = 0; j < 7; ++j)
      __builtin_amdgcn_global_load_lds((const u32*)(wsrc[j] + (size_t)st * 64 * (WP * 2)), (u32*)(sb + K1_XB + (w * 7 + j) * 1024), 16, 0, 0);
  };

  const int aoff = (wm * 64 + fr) * 128 + ((fg ^ ((fr >> 1) & 7)) << 4);
  int boff[7];
  {
    const int q = (lane & 15) >> 2, p = lane & 3;
#pragma unroll
    for (int nt = 0; nt < 7; ++nt) {
      const int t = wn * 7 + nt;
      boff[nt] = K1_XB + (8 * fg + q) * K1_WPITCH + ((2 * (t ^ (fg & 1)) + (p >> 1)) << 4) + ((p & 1) << 3);
    }
  }

  f32x4 acc[4][7];
#pragma unroll
  for (int mt = 0; mt < 4; ++mt)
#pragma unroll
    for (int nt = 0; nt < 7; ++nt) acc[mt][nt] = f32x4{0.f, 0.f, 0.f, 0.f};

  issue(0, 0); issue(1, 1);
  for (int it = 0; it < 9; ++it) {
    const int buf = it % 3;
    if (it < 8) WAIT_V(11); else WAIT_V(0);
    WAIT_L0();
    RAW_BAR();
    if (it + 2 < 9) issue(it + 2, (it + 2) % 3);
    lds_cptr sb = (lds_cptr)smem + buf * K1_STAGE;
#pragma unroll
    for (int s = 0; s < 2; ++s) {
      bf16x8 a[4], bb[7];
#pragma unroll
      for (int mt = 0; mt < 4; ++mt) a[mt] = *(const AS3 bf16x8*)(sb + (aoff ^ (s * 64)) + mt * 2048);
#pragma unroll
      for (int nt = 0; nt < 7; ++nt) {
        s16x4 lo = trread(sb + boff[nt] + s * (32 * K1_WPITCH));
        s16x4 hi = trread(sb + boff[nt] + s * (32 * K1_WPITCH) + 4 * K1_WPITCH);
        bb[nt] = cat8(lo, hi);
      }
#pragma unroll
      for (int mt = 0; mt < 4; ++mt)
#pragma unroll
        for (int nt = 0; nt < 7; ++nt) acc[mt][nt] = MFMA16(a[mt], bb[nt], acc[mt][nt]);
    }
  }

  __syncthreads();
  char* E = smem;
#pragma unroll
  for (int mt = 0; mt < 4; ++mt)
#pragma unroll
    for (int nt = 0; nt < 7; ++nt) {
      const int col = (wn * 7 + nt) * 16 + fr;
      const int row0 = wm * 64 + mt * 16 + fg * 4;
      float v0 = rbf(acc[mt][nt][0]), v1 = rbf(acc[mt][nt][1]), v2 = rbf(acc[mt][nt][2]), v3 = rbf(acc[mt][nt][3]);
      if (br == 0 && col >= 72 && col < 144) { v0 = phiA(v0); v1 = phiA(v1); v2 = phiA(v2); v3 = phiA(v3); }
      u32x2 pk = {pack2(v0, v1), pack2(v2, v3)};
      *(u32x2*)(E + col * EP + row0 * 2) = pk;
    }
  __syncthreads();

  lds_cptr Ec = (lds_cptr)smem;
  const int q4 = (lane & 15) >> 2, p4 = lane & 3;
  {
#pragma unroll
    for (int rbk = 0; rbk < 2; ++rbk)
#pragma unroll
      for (int c = 0; c < 3; ++c) {
        int ch = 4 * c + fg;
        const bool valid = ch < 9;
        if (!valid) ch = 8;
        const int R0 = w * 32 + rbk * 16;
        s16x4 lo = trread(Ec + (8 * ch + q4) * EP + (R0 + 4 * p4) * 2);
        s16x4 hi = trread(Ec + (8 * ch + 4 + q4) * EP + (R0 + 4 * p4) * 2);
        if (valid) {
          u16* dst = qbuf + (size_t)(rb * 128 + R0 + fr) * QP + br * DM + g4 * 72 + 8 * ch;
          *(bf16x8*)dst = cat8(lo, hi);
        }
      }
  }

  const int hh = w;
  const int head = g4 * 4 + hh;
  if (br == 1) {
    bf16x8 a0, a1;
    load_wphi_frags(wphi, head, lane, a0, a1);
    char* Phi = smem + PHI_OFF;
#pragma unroll 2
    for (int rt = 0; rt < 8; ++rt) {
      const int R0 = rt * 16;
      const int c0 = 72 + 18 * hh + 8 * fg;
      s16x4 lo = trread(Ec + (c0 + q4) * EP + (R0 + 4 * p4) * 2);
      s16x4 hi = trread(Ec + (c0 + 4 + q4) * EP + (R0 + 4 * p4) * 2);
      HFeat f;
      hedgehog(cat8(lo, hi), a0, a1, fg, f);
      const int tok = R0 + fr;
      u16* pcol = (u16*)(Phi + tok * 2);
#pragma unroll
      for (int i = 0; i < 4; ++i) {
        *(u16*)((char*)pcol + (hh * 36 + 4 * fg + i) * EP) = (u16)(pack2(f.fp[i], 0.f) & 0xffff);
        *(u16*)((char*)pcol + (hh * 36 + 18 + 4 * fg + i) * EP) = (u16)(pack2(f.fm[i], 0.f) & 0xffff);
      }
      if (fg == 0) {
        *(u16*)((char*)pcol + (hh * 36 + 16) * EP) = (u16)(pack2(f.xp[0], 0.f) & 0xffff);
        *(u16*)((char*)pcol + (hh * 36 + 17) * EP) = (u16)(pack2(f.xp[1], 0.f) & 0xffff);
        *(u16*)((char*)pcol + (hh * 36 + 34) * EP) = (u16)(pack2(f.xm[0], 0.f) & 0xffff);
        *(u16*)((char*)pcol + (hh * 36 + 35) * EP) = (u16)(pack2(f.xm[1], 0.f) & 0xffff);
      }
    }
    __syncthreads();
  }

  {
    f32x4 kvacc[3][2];
#pragma unroll
    for (int i = 0; i < 3; ++i) { kvacc[i][0] = f32x4{0.f, 0.f, 0.f, 0.f}; kvacc[i][1] = f32x4{0.f, 0.f, 0.f, 0.f}; }
    int vcol0 = 144 + 18 * hh + fr; int vcol1 = vcol0 + 16; if (vcol1 > E_ROWS - 1) vcol1 = E_ROWS - 1;
    const int abase = br ? (PHI_OFF + (hh * 36 + fr) * EP) : ((72 + 18 * hh + fr) * EP);
    const u32 one2 = 0x3f803f80u;
#pragma unroll
    for (int ks = 0; ks < 4; ++ks) {
      const int toff = (32 * ks + 8 * fg) * 2;
      bf16x8 vb0 = *(const AS3 bf16x8*)(Ec + vcol0 * EP + toff);
      bf16x8 vb1 = *(const AS3 bf16x8*)(Ec + vcol1 * EP + toff);
      if (fr == 2) vb1 = mk8(one2, one2, one2, one2);
#pragma unroll
      for (int mt = 0; mt < 3; ++mt) {
        if (mt == 2 && br == 0) continue;
        bf16x8 af = *(const AS3 bf16x8*)(Ec + abase + mt * 16 * EP + toff);
        kvacc[mt][0] = MFMA16(af, vb0, kvacc[mt][0]);
        kvacc[mt][1] = MFMA16(af, vb1, kvacc[mt][1]);
      }
    }
    float* dst = part + (size_t)(rb * NH + head) * PARTP;
    const int F = br ? 36 : 18;
    const int kvo = br ? KVB_OFF : KVA_OFF, zo = br ? ZB_OFF : ZA_OFF;
#pragma unroll
    for (int mt = 0; mt < 3; ++mt) {
#pragma unroll
      for (int i = 0; i < 4; ++i) {
        const int m = 16 * mt + 4 * fg + i;
        if (m < F) {
          dst[kvo + m * DH + fr] = kvacc[mt][0][i];
          if (fr < 2) dst[kvo + m * DH + 16 + fr] = kvacc[mt][1][i];
          if (fr == 2) dst[zo + m] = kvacc[mt][1][i];
        }
      }
    }
  }
}

__global__ __launch_bounds__(512)
void k2_attn(const u16* __restrict__ qbuf, const float* __restrict__ part, const u16* __restrict__ wphi,
             const u16* __restrict__ x, const u16* __restrict__ gate_w,
             u16* __restrict__ obuf, float* __restrict__ gbuf) {
  __shared__ __attribute__((aligned(16))) float red[PARTP];
  const int tid = threadIdx.x, lane = tid & 63;
  const int w = __builtin_amdgcn_readfirstlane(tid >> 6);
  const int b = blockIdx.x;
  const int xcd = b & 7, j = b >> 3;
  const int head = xcd * 4 + (j & 3), rr = j >> 2;
  const int fr = lane & 15, fg = lane >> 4;

  if (tid < PARTP / 4) {
    const float4* src = (const float4*)(part + (size_t)head * PARTP) + tid;
    float4 v[16];
#pragma unroll
    for (int r = 0; r < 16; ++r) v[r] = src[(size_t)r * NH * PARTP / 4];
    float4 s = v[0];
#pragma unroll
    for (int r = 1; r < 16; ++r) { s.x += v[r].x; s.y += v[r].y; s.z += v[r].z; s.w += v[r].w; }
    s.x = rbf(s.x); s.y = rbf(s.y); s.z = rbf(s.z); s.w = rbf(s.w);
    *(float4*)(red + tid * 4) = s;
  }

  {
    const int row = b * 8 + w;
    float s = 0.f;
#pragma unroll
    for (int i = 0; i < 9; ++i) {
      const int c = i * 64 + lane;
      s += rbf(bf2f(x[(size_t)row * DM + c]) * bf2f(gate_w[c]));
    }
#pragma unroll
    for (int o = 32; o >= 1; o >>= 1) s += __shfl_xor(s, o);
    s = rbf(s);
    float g = rbf(1.0f / (1.0f + __expf(-s)));
    if (lane == 0) gbuf[row] = g;
  }

  u32x4 qa[2], qb[2];
#pragma unroll
  for (int rt = 0; rt < 2; ++rt) {
    const int row = rr * 256 + w * 32 + rt * 16 + fr;
    const u32* pa = (const u32*)(qbuf + (size_t)row * QP + head * DH + 8 * fg);
    const u32* pb = (const u32*)(qbuf + (size_t)row * QP + DM + head * DH + 8 * fg);
    u32x4 va = {0, 0, 0, 0}, vb = {0, 0, 0, 0};
    if (fg < 2) { va = u32x4{pa[0], pa[1], pa[2], pa[3]}; vb = u32x4{pb[0], pb[1], pb[2], pb[3]}; }
    else if (fg == 2) { va[0] = pa[0]; vb[0] = pb[0]; }
    qa[rt] = va; qb[rt] = vb;
  }
  bf16x8 wa0, wa1;
  load_wphi_frags(wphi, head, lane, wa0, wa1);

  __syncthreads();

  auto kvval = [&](int off_kv, int off_z, int F, int Fmax, int mt) -> float {
    if (F >= Fmax) return 0.f;
    if (mt == 0) return red[off_kv + F * DH + fr];
    if (fr < 2) return red[off_kv + F * DH + 16 + fr];
    if (fr == 2) return red[off_z + F];
    return 0.f;
  };
  bf16x8 akA[2];
#pragma unroll
  for (int mt = 0; mt < 2; ++mt) {
    u32 r[4];
#pragma unroll
    for (int jj = 0; jj < 4; ++jj)
      r[jj] = pack2(kvval(KVA_OFF, ZA_OFF, 8 * fg + 2 * jj, 18, mt), kvval(KVA_OFF, ZA_OFF, 8 * fg + 2 * jj + 1, 18, mt));
    akA[mt] = mk8(r[0], r[1], r[2], r[3]);
  }
  bf16x8 akB[2][2];
#pragma unroll
  for (int mt = 0; mt < 2; ++mt) {
    u32 r[4];
    r[0] = pack2(kvval(KVB_OFF, ZB_OFF, 4 * fg + 0, 36, mt), kvval(KVB_OFF, ZB_OFF, 4 * fg + 1, 36, mt));
    r[1] = pack2(kvval(KVB_OFF, ZB_OFF, 4 * fg + 2, 36, mt), kvval(KVB_OFF, ZB_OFF, 4 * fg + 3, 36, mt));
    r[2] = pack2(kvval(KVB_OFF, ZB_OFF, 18 + 4 * fg + 0, 36, mt), kvval(KVB_OFF, ZB_OFF, 18 + 4 * fg + 1, 36, mt));
    r[3] = pack2(kvval(KVB_OFF, ZB_OFF, 18 + 4 * fg + 2, 36, mt), kvval(KVB_OFF, ZB_OFF, 18 + 4 * fg + 3, 36, mt));
    akB[mt][0] = mk8(r[0], r[1], r[2], r[3]);
    u32 s0 = 0, s1 = 0;
    if (fg == 0) {
      s0 = pack2(kvval(KVB_OFF, ZB_OFF, 16, 36, mt), kvval(KVB_OFF, ZB_OFF, 17, 36, mt));
      s1 = pack2(kvval(KVB_OFF, ZB_OFF, 34, 36, mt), kvval(KVB_OFF, ZB_OFF, 35, 36, mt));
    }
    akB[mt][1] = mk8(s0, s1, 0, 0);
  }

#pragma unroll
  for (int rt = 0; rt < 2; ++rt) {
    const int row = rr * 256 + w * 32 + rt * 16 + fr;
    {
      u32 pr[4];
#pragma unroll
      for (int jj = 0; jj < 4; ++jj) {
        const int f = 8 * fg + 2 * jj;
        float t0 = lo2f(qa[rt][jj]), t1 = hi2f(qa[rt][jj]);
        float v0 = (f < DH) ? phiA(t0) : 0.f, v1 = (f + 1 < DH) ? phiA(t1) : 0.f;
        pr[jj] = pack2(v0, v1);
      }
      bf16x8 pq = mk8(pr[0], pr[1], pr[2], pr[3]);
      f32x4 z4 = {0.f, 0.f, 0.f, 0.f};
      f32x4 d0 = MFMA16(akA[0], pq, z4);
      f32x4 d1 = MFMA16(akA[1], pq, z4);
      float den = __shfl(d1[2], fr);
      den = rbf(rbf(den) + 1e-6f);
      float o0 = rbf(rbf(d0[0]) / den), o1 = rbf(rbf(d0[1]) / den), o2 = rbf(rbf(d0[2]) / den), o3 = rbf(rbf(d0[3]) / den);
      u16* dst = obuf + (size_t)row * QP + head * DH;
      *(u32*)(dst + 4 * fg) = pack2(o0, o1);
      *(u32*)(dst + 4 * fg + 2) = pack2(o2, o3);
      if (fg == 0) *(u32*)(dst + 16) = pack2(rbf(rbf(d1[0]) / den), rbf(rbf(d1[1]) / den));
    }
    {
      HFeat f;
      hedgehog(__builtin_bit_cast(bf16x8, qb[rt]), wa0, wa1, fg, f);
      bf16x8 f0 = mk8(pack2(f.fp[0], f.fp[1]), pack2(f.fp[2], f.fp[3]), pack2(f.fm[0], f.fm[1]), pack2(f.fm[2], f.fm[3]));
      u32 s0 = 0, s1 = 0;
      if (fg == 0) { s0 = pack2(f.xp[0], f.xp[1]); s1 = pack2(f.xm[0], f.xm[1]); }
      bf16x8 f1 = mk8(s0, s1, 0, 0);
      f32x4 z4 = {0.f, 0.f, 0.f, 0.f};
      f32x4 d0 = MFMA16(akB[0][0], f0, z4); d0 = MFMA16(akB[0][1], f1, d0);
      f32x4 d1 = MFMA16(akB[1][0], f0, z4); d1 = MFMA16(akB[1][1], f1, d1);
      float den = __shfl(d1[2], fr);
      den = rbf(rbf(den) + 1e-6f);
      float o0 = rbf(rbf(d0[0]) / den), o1 = rbf(rbf(d0[1]) / den), o2 = rbf(rbf(d0[2]) / den), o3 = rbf(rbf(d0[3]) / den);
      u16* dst = obuf + (size_t)row * QP + DM + head * DH;
      *(u32*)(dst + 4 * fg) = pack2(o0, o1);
      *(u32*)(dst + 4 * fg + 2) = pack2(o2, o3);
      if (fg == 0) *(u32*)(dst + 16) = pack2(rbf(rbf(d1[0]) / den), rbf(rbf(d1[1]) / den));
    }
  }
}

constexpr int K3_AB = 4096;
constexpr int K3_BP = 192;
constexpr int K3_STG = K3_AB + 32 * K3_BP;
constexpr int K3_RP = 68;

__global__ __launch_bounds__(256, 1)
void k3_out(const u16* __restrict__ obuf, const u16* __restrict__ woA, const u16* __restrict__ woB,
            const float* __restrict__ gbuf, u16* __restrict__ out) {
  __shared__ __attribute__((aligned(16))) char smem[4 * 3 * K3_STG];
  static_assert(4 * 80 * K3_RP * 4 <= 4 * 3 * K3_STG, "k3 lds");
  const int tid = threadIdx.x, lane = tid & 63;
  const int w = __builtin_amdgcn_readfirstlane(tid >> 6);
  const int b = blockIdx.x;
  const int xcd = b & 7, j = b >> 3;
  const int rbk = xcd * 4 + (j & 3), cb = j >> 2;
  const int br = w >> 1, kh = w & 1;
  const u16* __restrict__ Wo = br ? woB : woA;
  const int fr = lane & 15, fg = lane >> 4;

  const char* asrc[4];
#pragma unroll
  for (int jj = 0; jj < 4; ++jj) {
    const int rl = 16 * jj + (lane >> 2);
    const int chunk = (lane & 3) ^ ((4 - ((rl >> 2) & 3)) & 3);
    asrc[jj] = (const char*)obuf + ((size_t)(rbk * 64 + rl) * QP + br * DM + kh * 288 + chunk * 8) * 2;
  }
  const char* bsrc[6];
#pragma unroll
  for (int jj = 0; jj < 6; ++jj) {
    const int slot = jj * 64 + lane;
    const int kk = slot / 12, pos = slot % 12;
    int u = pos ^ (((kk >> 3) & 1) << 1);
    if (u > 8) u = 8;
    bsrc[jj] = (const char*)Wo + ((size_t)(kh * 288 + kk) * DM + cb * 72 + u * 8) * 2;
  }
  char* wsm = smem + w * 3 * K3_STG;
  auto issue = [&](int st, int buf) {
    char* sb = wsm + buf * K3_STG;
#pragma unroll
    for (int jj = 0; jj < 4; ++jj)
      __builtin_amdgcn_global_load_lds((const u32*)(asrc[jj] + st * 64), (u32*)(sb + jj * 1024), 16, 0, 0);
#pragma unroll
    for (int jj = 0; jj < 6; ++jj)
      __builtin_amdgcn_global_load_lds((const u32*)(bsrc[jj] + (size_t)st * 32 * DM * 2), (u32*)(sb + K3_AB + jj * 1024), 16, 0, 0);
  };
  int aoff[4];
#pragma unroll
  for (int mt = 0; mt < 4; ++mt) {
    const int row = 16 * mt + fr;
    aoff[mt] = row * 64 + ((fg ^ ((4 - ((row >> 2) & 3)) & 3)) << 4);
  }
  int boff[5];
  {
    const int q = (lane & 15) >> 2, p = lane & 3;
#pragma unroll
    for (int nt = 0; nt < 5; ++nt)
      boff[nt] = K3_AB + (8 * fg + q) * K3_BP + ((2 * (nt ^ (fg & 1)) + (p >> 1)) << 4) + ((p & 1) << 3);
  }
  f32x4 acc[4][5];
#pragma unroll
  for (int mt = 0; mt < 4; ++mt)
#pragma unroll
    for (int nt = 0; nt < 5; ++nt) acc[mt][nt] = f32x4{0.f, 0.f, 0.f, 0.f};

  issue(0, 0); issue(1, 1);
  for (int st = 0; st < 9; ++st) {
    const int buf = st % 3;
    if (st < 8) WAIT_V(10); else WAIT_V(0);
    WAIT_L0();
    if (st + 2 < 9) issue(st + 2, (st + 2) % 3);
    lds_cptr sb = (lds_cptr)wsm + buf * K3_STG;
    bf16x8 a[4], bb[5];
#pragma unroll
    for (int mt = 0; mt < 4; ++mt) a[mt] = *(const AS3 bf16x8*)(sb + aoff[mt]);
#pragma unroll
    for (int nt = 0; nt < 5; ++nt) {
      s16x4 lo = trread(sb + boff[nt]);
      s16x4 hi = trread(sb + boff[nt] + 4 * K3_BP);
      bb[nt] = cat8(lo, hi);
    }
#pragma unroll
    for (int mt = 0; mt < 4; ++mt)
#pragma unroll
      for (int nt = 0; nt < 5; ++nt) acc[mt][nt] = MFMA16(a[mt], bb[nt], acc[mt][nt]);
  }

  __syncthreads();
  float* red = (float*)smem;
#pragma unroll
  for (int mt = 0; mt < 4; ++mt)
#pragma unroll
    for (int nt = 0; nt < 5; ++nt) {
      const int col = 16 * nt + fr, row0 = 16 * mt + 4 * fg;
      *(f32x4*)(red + (w * 80 + col) * K3_RP + row0) = acc[mt][nt];
    }
  __syncthreads();
  for (int task = tid; task < 64 * 9; task += 256) {
    const int r = task & 63, c = task >> 6;
    const float g = gbuf[rbk * 64 + r];
    const float omg = rbf(1.0f - g);
    u32 o[4];
#pragma unroll
    for (int e2 = 0; e2 < 4; ++e2) {
      float res[2];
#pragma unroll
      for (int h2 = 0; h2 < 2; ++h2) {
        const int col = 8 * c + 2 * e2 + h2;
        const float av = rbf(red[(0 * 80 + col) * K3_RP + r] + red[(1 * 80 + col) * K3_RP + r]);
        const float bv = rbf(red[(2 * 80 + col) * K3_RP + r] + red[(3 * 80 + col) * K3_RP + r]);
        res[h2] = rbf(rbf(g * av) + rbf(omg * bv));
      }
      o[e2] = pack2(res[0], res[1]);
    }
    u32x4 ov = {o[0], o[1], o[2], o[3]};
    *(u32x4*)(out + (size_t)(rbk * 64 + r) * DM + cb * 72 + 8 * c) = ov;
  }
}

extern "C" void kernel_launch(void* const* d_in, const int* in_sizes, int n_in,
                              void* d_out, int out_size, void* d_ws, size_t ws_size,
                              hipStream_t stream) {
  const u16* x    = (const u16*)d_in[0];
  const u16* wA   = (const u16*)d_in[1];
  const u16* woA  = (const u16*)d_in[2];
  const u16* wB   = (const u16*)d_in[4];
  const u16* wphi = (const u16*)d_in[5];
  const u16* woB  = (const u16*)d_in[6];
  const u16* gw   = (const u16*)d_in[7];
  char* ws = (char*)d_ws;
  u16*   qbuf = (u16*)(ws);
  float* part = (float*)(ws + 4718592);
  u16*   obuf = (u16*)(ws + 6823936);
  float* gbuf = (float*)(ws + 11542528);
  k1_qkv<<<256, 256, 0, stream>>>(x, wA, wB, wphi, qbuf, part);
  k2_attn<<<256, 512, 0, stream>>>(qbuf, part, wphi, x, gw, obuf, gbuf);
  k3_out<<<256, 256, 0, stream>>>(obuf, woA, woB, gbuf, (u16*)d_out);
}
```

```cpp
#include <hip/hip_runtime.h>
#include <hip/hip_bf16.h>
#include <stdint.h>

typedef unsigned short u16;
typedef unsigned int u32;
typedef __attribute__((ext_vector_type(8))) short bf16x8;
typedef __attribute__((ext_vector_type(4))) short s16x4;
typedef __attribute__((ext_vector_type(4))) float f32x4;
typedef __attribute__((ext_vector_type(4))) unsigned u32x4;
typedef __attribute__((ext_vector_type(2))) unsigned u32x2;
typedef __attribute__((ext_vector_type(2))) __bf16 bf16x2n;
typedef __attribute__((address_space(3))) const char* lds_cptr;
typedef __attribute__((address_space(3))) char* lds_ptr;
typedef short v4i16_t __attribute__((ext_vector_type(4)));
#define AS3 __attribute__((address_space(3)))
#define DEV __device__ __forceinline__

constexpr int SEQ = 2048, DM = 576, NH = 32, DH = 18;
constexpr int QP = 1152;
constexpr int WP = 1728;
constexpr int PARTP = 1028;
constexpr int KVA_OFF = 0, ZA_OFF = 324, KVB_OFF = 344, ZB_OFF = 992;

DEV float bf2f(u16 h) { return __uint_as_float(((u32)h) << 16); }
DEV float rbf(float f) { return (float)(__bf16)f; }
DEV u32 pack2(float lo, float hi) { bf16x2n v = {(__bf16)lo, (__bf16)hi}; return __builtin_bit_cast(u32, v); }
DEV float lo2f(u32 p) { return __uint_as_float(p << 16); }
DEV float hi2f(u32 p) { return __uint_as_float(p & 0xffff0000u); }
DEV s16x4 trread(lds_cptr p) { return __builtin_bit_cast(s16x4, __builtin_amdgcn_ds_read_tr16_b64_v4i16((AS3 v4i16_t*)p)); }
DEV bf16x8 cat8(s16x4 lo, s16x4 hi) { return __builtin_shufflevector(lo, hi, 0, 1, 2, 3, 4, 5, 6, 7); }
DEV bf16x8 mk8(u32 a, u32 b, u32 c, u32 d) { u32x4 v = {a, b, c, d}; return __builtin_bit_cast(bf16x8, v); }
DEV void glds16(const void* g, u32 lds_base) {
  u32 sv;
  asm volatile("s_mov_b32 %0, m0\n\ts_mov_b32 m0, %2\n\ts_nop 0\n\tglobal_load_lds_dwordx4 %1, off\n\ts_mov_b32 m0, %0"
               : "=&s"(sv) : "v"(g), "s"(lds_base) : "memory");
}
#define MFMA16(a, b, c) __builtin_amdgcn_mfma_f32_16x16x32_bf16((a), (b), (c), 0, 0, 0)
#define WAIT_V(n) asm volatile("s_waitcnt vmcnt(" #n ")" ::: "memory")
#define WAIT_L0() asm volatile("s_waitcnt lgkmcnt(0)" ::: "memory")
#define RAW_BAR() __builtin_amdgcn_s_barrier()
#define STAMP2(i)
#define STAMP2RT(i)
#define STAMP(i)
#define STAMPRT(i)

DEV float xl_max(float v) {
  auto r = __builtin_amdgcn_permlane16_swap(__float_as_uint(v), __float_as_uint(v), false, false);
  v = fmaxf(__uint_as_float(r[0]), __uint_as_float(r[1]));
  auto s = __builtin_amdgcn_permlane32_swap(__float_as_uint(v), __float_as_uint(v), false, false);
  return fmaxf(__uint_as_float(s[0]), __uint_as_float(s[1]));
}
DEV float xl_min(float v) {
  auto r = __builtin_amdgcn_permlane16_swap(__float_as_uint(v), __float_as_uint(v), false, false);
  v = fminf(__uint_as_float(r[0]), __uint_as_float(r[1]));
  auto s = __builtin_amdgcn_permlane32_swap(__float_as_uint(v), __float_as_uint(v), false, false);
  return fminf(__uint_as_float(s[0]), __uint_as_float(s[1]));
}
DEV float xl_sum(float v) {
  auto r = __builtin_amdgcn_permlane16_swap(__float_as_uint(v), __float_as_uint(v), false, false);
  v = __uint_as_float(r[0]) + __uint_as_float(r[1]);
  auto s = __builtin_amdgcn_permlane32_swap(__float_as_uint(v), __float_as_uint(v), false, false);
  return __uint_as_float(s[0]) + __uint_as_float(s[1]);
}

DEV float phiA(float t) {
  float u = t > 0.f ? rbf(t + 1.0f) : rbf(__expf(t));
  return rbf(u + 1e-6f);
}

struct HFeat { float fp[4], fm[4], xp[2], xm[2]; };
DEV void hedgehog(bf16x8 raw, bf16x8 a0, bf16x8 a1, int g, HFeat& o) {
  f32x4 z4 = {0.f, 0.f, 0.f, 0.f};
  f32x4 d0 = MFMA16(a0, raw, z4);
  f32x4 d1 = MFMA16(a1, raw, z4);
  float p0 = rbf(d0[0]), p1 = rbf(d0[1]), p2 = rbf(d0[2]), p3 = rbf(d0[3]);
  float e0 = rbf(d1[0]), e1 = rbf(d1[1]);
  float mx = fmaxf(fmaxf(p0, p1), fmaxf(p2, p3));
  float mn = fminf(fminf(p0, p1), fminf(p2, p3));
  if (g == 0) { mx = fmaxf(mx, fmaxf(e0, e1)); mn = fminf(mn, fminf(e0, e1)); }
  mx = xl_max(mx);
  mn = xl_min(mn);
  float a[4] = {__expf(p0 - mx), __expf(p1 - mx), __expf(p2 - mx), __expf(p3 - mx)};
  float b[4] = {__expf(mn - p0), __expf(mn - p1), __expf(mn - p2), __expf(mn - p3)};
  float xa0 = __expf(e0 - mx), xa1 = __expf(e1 - mx), xb0 = __expf(mn - e0), xb1 = __expf(mn - e1);
  float sp = (a[0] + a[1]) + (a[2] + a[3]);
  float sm = (b[0] + b[1]) + (b[2] + b[3]);
  if (g == 0) { sp += xa0 + xa1; sm += xb0 + xb1; }
  sp = xl_sum(sp);
  sm = xl_sum(sm);
  float ip = 1.0f / sp, im = 1.0f / sm;
#pragma unroll
  for (int i = 0; i < 4; ++i) { o.fp[i] = rbf(a[i] * ip); o.fm[i] = rbf(b[i] * im); }
  o.xp[0] = rbf(xa0 * ip); o.xp[1] = rbf(xa1 * ip);
  o.xm[0] = rbf(xb0 * im); o.xm[1] = rbf(xb1 * im);
}

DEV void load_wphi_frags(const u16* __restrict__ wphi, int head, int lane, bf16x8& a0, bf16x8& a1) {
  const int m = lane & 15, g = lane >> 4;
  u32 w0[4], w1[4];
#pragma unroll
  for (int jj = 0; jj < 4; ++jj) {
    u32 v0 = 0, v1 = 0;
#pragma unroll
    for (int h2 = 0; h2 < 2; ++h2) {
      const int f = 8 * g + 2 * jj + h2;
      u32 e0 = 0, e1 = 0;
      if (f < DH) {
        e0 = wphi[(head * DH + f) * DH + m];
        if (m < 2) e1 = wphi[(head * DH + f) * DH + 16 + m];
      }
      v0 |= e0 << (16 * h2); v1 |= e1 << (16 * h2);
    }
    w0[jj] = v0; w1[jj] = v1;
  }
  a0 = mk8(w0[0], w0[1], w0[2], w0[3]);
  a1 = mk8(w1[0], w1[1], w1[2], w1[3]);
}

constexpr int K1_XB = 16384;
constexpr int K1_WPITCH = 448;
constexpr int K1_STAGE = K1_XB + 64 * K1_WPITCH;
constexpr int EP = 272;
constexpr int E_ROWS = 224;
constexpr int PHI_OFF = E_ROWS * EP;
constexpr int T2_OFF = PHI_OFF + 160 * EP;
constexpr int T2P = 208;
constexpr int K1_WPHI_OFF = 3 * K1_STAGE;
static_assert(T2_OFF + 128 * T2P <= 3 * K1_STAGE, "k1 lds");

__global__ __launch_bounds__(512, 1)
void k1_qkv(const u16* __restrict__ x, const u16* __restrict__ wA, const u16* __restrict__ wB,
            const u16* __restrict__ wphi, const u16* __restrict__ gate_w,
            u16* __restrict__ qbuf, float* __restrict__ part, float* __restrict__ gbuf
            ) {
  __shared__ __attribute__((aligned(16))) char smem[3 * K1_STAGE + 2624];
  const int tid = threadIdx.x, lane = tid & 63;
  const int w = __builtin_amdgcn_readfirstlane(tid >> 6);
  const int b = blockIdx.x;
  const int g4 = b & 7, br = (b >> 3) & 1, rb = b >> 4;
  const u16* __restrict__ W = br ? wB : wA;
  const int wm = w >> 1, wn = w & 1;
  const int fr = lane & 15, fg = lane >> 4;
  STAMPRT(30); STAMP(0);

  const u32 lds0 = (u32)(uintptr_t)smem;
  const char* xsrc[2];
#pragma unroll
  for (int j = 0; j < 2; ++j) {
    const int rl = (w * 2 + j) * 8 + (lane >> 3);
    const int chunk = (lane & 7) ^ ((rl >> 1) & 7);
    xsrc[j] = (const char*)x + (size_t)(rb * 128 + rl) * (DM * 2) + chunk * 16;
  }
  const int wi0 = (w < 4) ? 4 * w : 16 + 3 * (w - 4);
  const char* wsrc[4];
#pragma unroll
  for (int j = 0; j < 4; ++j) {
    int piece = wi0 + j; if (piece > 27) piece = 27;
    const int slot = piece * 64 + lane;
    const int k = slot / 28, pos = slot % 28;
    int u = pos ^ (((k >> 3) & 1) << 1);
    if (u > 26) u = 26;
    const int seg = u / 9, wi = u % 9;
    wsrc[j] = (const char*)W + (size_t)k * (WP * 2) + (seg * DM + g4 * 72 + wi * 8) * 2;
  }
  auto issue = [&](int st, int buf) {
    const u32 sb = lds0 + buf * K1_STAGE;
#pragma unroll
    for (int j = 0; j < 2; ++j)
      glds16(xsrc[j] + st * 128, __builtin_amdgcn_readfirstlane(sb + (w * 2 + j) * 1024));
#pragma unroll
    for (int j = 0; j < 3; ++j)
      glds16(wsrc[j] + (size_t)st * 64 * (WP * 2), __builtin_amdgcn_readfirstlane(sb + K1_XB + (wi0 + j) * 1024));
    if (w < 4) glds16(wsrc[3] + (size_t)st * 64 * (WP * 2), __builtin_amdgcn_readfirstlane(sb + K1_XB + (wi0 + 3) * 1024));
  };
  STAMP(1);
  issue(0, 0); issue(1, 1);

  {
    const int row = b * 8 + w;
    float sg = 0.f;
#pragma unroll
    for (int i = 0; i < 9; ++i) sg += rbf(bf2f(x[(size_t)row * DM + i * 64 + lane]) * bf2f(gate_w[i * 64 + lane]));
    sg = xl_sum(sg);
    sg += __builtin_amdgcn_update_dpp(0.f, sg, 0x128, 0xf, 0xf, true);
    sg += __builtin_amdgcn_update_dpp(0.f, sg, 0x124, 0xf, 0xf, true);
    sg += __builtin_amdgcn_update_dpp(0.f, sg, 0x122, 0xf, 0xf, true);
    sg += __builtin_amdgcn_update_dpp(0.f, sg, 0x121, 0xf, 0xf, true);
    sg = rbf(sg);
    const float g = rbf(1.0f / (1.0f + __expf(-sg)));
    if (lane == 0) gbuf[row] = g;
  }

  if (br == 1) {
    const u32* src = (const u32*)(wphi + (size_t)g4 * 4 * DH * DH);
    for (int i = tid; i < 648; i += 512) *(u32*)(smem + K1_WPHI_OFF + i * 4) = src[i];
  }

  const int aoff = (wm * 32 + fr) * 128 + ((fg ^ ((fr >> 1) & 7)) << 4);
  int boff[7];
  {
    const int q = (lane & 15) >> 2, p = lane & 3;
#pragma unroll
    for (int nt = 0; nt < 7; ++nt) {
      const int t = wn * 7 + nt;
      boff[nt] = K1_XB + (8 * fg + q) * K1_WPITCH + ((2 * (t ^ (fg & 1)) + (p >> 1)) << 4) + ((p & 1) << 3);
    }
  }

  f32x4 acc[2][7];
#pragma unroll
  for (int mt = 0; mt < 2; ++mt)
#pragma unroll
    for (int nt = 0; nt < 7; ++nt) acc[mt][nt] = f32x4{0.f, 0.f, 0.f, 0.f};

#pragma unroll
  for (int it = 0; it < 9; ++it) {
    const int buf = it % 3;
    if (it < 8) { if (w < 4) WAIT_V(6); else WAIT_V(5); } else WAIT_V(0);
    WAIT_L0();
    RAW_BAR();
    STAMP(2 + it);
    if (it + 2 < 9) issue(it + 2, (it + 2) % 3);
    lds_cptr sb = (lds_cptr)smem + buf * K1_STAGE;
#pragma unroll
    for (int s = 0; s < 2; ++s) {
      bf16x8 a[2], bb[7];
#pragma unroll
      for (int mt = 0; mt < 2; ++mt) a[mt] = *(const AS3 bf16x8*)(sb + (aoff ^ (s * 64)) + mt * 2048);
#pragma unroll
      for (int nt = 0; nt < 7; ++nt) {
        s16x4 lo = trread(sb + boff[nt] + s * (32 * K1_WPITCH));
        s16x4 hi = trread(sb + boff[nt] + s * (32 * K1_WPITCH) + 4 * K1_WPITCH);
        bb[nt] = cat8(lo, hi);
      }
#pragma unroll
      for (int mt = 0; mt < 2; ++mt)
#pragma unroll
        for (int nt = 0; nt < 7; ++nt) acc[mt][nt] = MFMA16(a[mt], bb[nt], acc[mt][nt]);
    }
  }
  STAMP(11);

#pragma unroll
  for (int mt = 0; mt < 2; ++mt)
#pragma unroll
    for (int nt = 0; nt < 7; ++nt)
#pragma unroll
      for (int i = 0; i < 4; ++i) acc[mt][nt][i] = rbf(acc[mt][nt][i]);
  if (br == 0) {
    if (wn == 0) {
#pragma unroll
      for (int mt = 0; mt < 2; ++mt)
#pragma unroll
        for (int i = 0; i < 4; ++i) {
          acc[mt][4][i] = (fr >= 8) ? phiA(acc[mt][4][i]) : acc[mt][4][i];
          acc[mt][5][i] = phiA(acc[mt][5][i]);
          acc[mt][6][i] = phiA(acc[mt][6][i]);
        }
    } else {
#pragma unroll
      for (int mt = 0; mt < 2; ++mt)
#pragma unroll
        for (int i = 0; i < 4; ++i) {
          acc[mt][0][i] = phiA(acc[mt][0][i]);
          acc[mt][1][i] = phiA(acc[mt][1][i]);
        }
    }
  }
  __syncthreads();
  char* E = smem;
#pragma unroll
  for (int mt = 0; mt < 2; ++mt)
#pragma unroll
    for (int nt = 0; nt < 7; ++nt) {
      const int col = (wn * 7 + nt) * 16 + fr;
      const int row0 = wm * 32 + mt * 16 + fg * 4;
      u32x2 pk = {pack2(acc[mt][nt][0], acc[mt][nt][1]), pack2(acc[mt][nt][2], acc[mt][nt][3])};
      *(u32x2*)(E + col * EP + row0 * 2) = pk;
    }
  __syncthreads();
  STAMP(13);

  lds_cptr Ec = (lds_cptr)smem;
  const int q4 = (lane & 15) >> 2, p4 = lane & 3;
  {
#pragma unroll
    for (int c = 0; c < 3; ++c) {
      int ch = 4 * c + fg;
      const bool valid = ch < 9;
      if (!valid) ch = 8;
      const int R0 = w * 16;
      s16x4 lo = trread(Ec + (8 * ch + q4) * EP + (R0 + 4 * p4) * 2);
      s16x4 hi = trread(Ec + (8 * ch + 4 + q4) * EP + (R0 + 4 * p4) * 2);
      if (valid) {
        u16* dst = qbuf + (size_t)(rb * 128 + R0 + fr) * QP + br * DM + g4 * 72 + 8 * ch;
        *(bf16x8*)dst = cat8(lo, hi);
      }
    }
  }
  STAMP(14);

  const int hh = w >> 1, half = w & 1;
  const int head = g4 * 4 + hh;
  if (br == 1) {
    bf16x8 a0, a1;
    {
      const u16* wp = (const u16*)(smem + K1_WPHI_OFF) + hh * DH * DH;
      u32 w0[4], w1[4];
#pragma unroll
      for (int jj = 0; jj < 4; ++jj) {
        u32 v0 = 0, v1 = 0;
#pragma unroll
        for (int h2 = 0; h2 < 2; ++h2) {
          const int f = 8 * fg + 2 * jj + h2;
          const int fc = f < DH ? f : 0;
          u32 e0 = wp[fc * DH + fr], e1 = wp[fc * DH + 16 + (fr & 1)];
          e0 = f < DH ? e0 : 0; e1 = (f < DH && fr < 2) ? e1 : 0;
          v0 |= e0 << (16 * h2); v1 |= e1 << (16 * h2);
        }
        w0[jj] = v0; w1[jj] = v1;
      }
      a0 = mk8(w0[0], w0[1], w0[2], w0[3]); a1 = mk8(w1[0], w1[1], w1[2], w1[3]);
    }
    char* T2 = smem + T2_OFF;
#pragma unroll
    for (int r4 = 0; r4 < 4; ++r4) {
      const int R0 = (half * 4 + r4) * 16;
      const int c0 = 72 + 18 * hh + 8 * fg;
      s16x4 lo = trread(Ec + (c0 + q4) * EP + (R0 + 4 * p4) * 2);
      s16x4 hi = trread(Ec + (c0 + 4 + q4) * EP + (R0 + 4 * p4) * 2);
      const bf16x8 kb = cat8(lo, hi);
      f32x4 z4 = {0.f, 0.f, 0.f, 0.f};
      f32x4 d0 = MFMA16(a0, kb, z4);
      f32x4 d1 = MFMA16(a1, kb, z4);
      const int tok = R0 + fr;
      u32x2 pk = {pack2(rbf(d0[0]), rbf(d0[1])), pack2(rbf(d0[2]), rbf(d0[3]))};
      *(u32x2*)(T2 + tok * T2P + hh * 48 + fg * 8) = pk;
      if (fg == 0) *(u32*)(T2 + tok * T2P + hh * 48 + 32) = pack2(rbf(d1[0]), rbf(d1[1]));
    }
    __syncthreads();
    {
      const int tok = tid & 127, h4 = tid >> 7;
      const u32* src = (const u32*)(smem + T2_OFF + tok * T2P + h4 * 48);
      u32x4 v0 = *(const u32x4*)src, v1 = *(const u32x4*)(src + 4);
      u32 v2 = src[8];
      float p[18];
#pragma unroll
      for (int i = 0; i < 4; ++i) { p[2 * i] = lo2f(v0[i]); p[2 * i + 1] = hi2f(v0[i]); p[8 + 2 * i] = lo2f(v1[i]); p[8 + 2 * i + 1] = hi2f(v1[i]); }
      p[16] = lo2f(v2); p[17] = hi2f(v2);
      float mx = p[0], mn = p[0];
#pragma unroll
      for (int i = 1; i < 18; ++i) { mx = fmaxf(mx, p[i]); mn = fminf(mn, p[i]); }
      float ep[18], em[18], sp = 0.f, sm = 0.f;
#pragma unroll
      for (int i = 0; i < 18; ++i) { ep[i] = __expf(p[i] - mx); em[i] = __expf(mn - p[i]); sp += ep[i]; sm += em[i]; }
      const float ip = 1.0f / sp, im = 1.0f / sm;
      char* dst = smem + PHI_OFF + (h4 * 36) * EP + tok * 2;
#pragma unroll
      for (int i = 0; i < 18; ++i) {
        *(u16*)(dst + i * EP) = (u16)(pack2(rbf(ep[i] * ip), 0.f) & 0xffff);
        *(u16*)(dst + (18 + i) * EP) = (u16)(pack2(rbf(em[i] * im), 0.f) & 0xffff);
      }
    }
    __syncthreads();
  }
  STAMP(15);

  {
    f32x4 kv0[2], kv1[2];
#pragma unroll
    for (int i = 0; i < 2; ++i) { kv0[i] = f32x4{0.f, 0.f, 0.f, 0.f}; kv1[i] = f32x4{0.f, 0.f, 0.f, 0.f}; }
    int vcol0 = 144 + 18 * hh + fr; int vcol1 = vcol0 + 16; if (vcol1 > E_ROWS - 1) vcol1 = E_ROWS - 1;
    const int abase = br ? (PHI_OFF + (hh * 36 + fr) * EP) : ((72 + 18 * hh + fr) * EP);
    const bool two = (br == 1) && (half == 0);
    const u32 one2 = 0x3f803f80u;
#pragma unroll
    for (int ks = 0; ks < 4; ++ks) {
      const int toff = (32 * ks + 8 * fg) * 2;
      bf16x8 vb0 = *(const AS3 bf16x8*)(Ec + vcol0 * EP + toff);
      bf16x8 vb1 = *(const AS3 bf16x8*)(Ec + vcol1 * EP + toff);
      if (fr == 2) vb1 = mk8(one2, one2, one2, one2);
      bf16x8 af = *(const AS3 bf16x8*)(Ec + abase + half * 16 * EP + toff);
      kv0[0] = MFMA16(af, vb0, kv0[0]);
      kv0[1] = MFMA16(af, vb1, kv0[1]);
      if (two) {
        bf16x8 af2 = *(const AS3 bf16x8*)(Ec + abase + 32 * EP + toff);
        kv1[0] = MFMA16(af2, vb0, kv1[0]);
        kv1[1] = MFMA16(af2, vb1, kv1[1]);
      }
    }
    float* dst = part + (size_t)(rb * NH + head) * PARTP;
    const int F = br ? 36 : 18;
    const int kvo = br ? KVB_OFF : KVA_OFF, zo = br ? ZB_OFF : ZA_OFF;
#pragma unroll
    for (int i = 0; i < 4; ++i) {
      const int m = 16 * half + 4 * fg + i;
      if (m < F) {
        dst[kvo + m * DH + fr] = kv0[0][i];
        if (fr < 2) dst[kvo + m * DH + 16 + fr] = kv0[1][i];
        if (fr == 2) dst[zo + m] = kv0[1][i];
      }
    }
    if (two) {
#pragma unroll
      for (int i = 0; i < 4; ++i) {
        const int m = 32 + 4 * fg + i;
        if (m < F) {
          dst[kvo + m * DH + fr] = kv1[0][i];
          if (fr < 2) dst[kvo + m * DH + 16 + fr] = kv1[1][i];
          if (fr == 2) dst[zo + m] = kv1[1][i];
        }
      }
    }
  }
  STAMP(12); STAMPRT(31);
}

__global__ __launch_bounds__(512)
void k2_attn(const u16* __restrict__ qbuf, const float* __restrict__ part, const u16* __restrict__ wphi,
             u16* __restrict__ obuf
             ) {
  __shared__ __attribute__((aligned(16))) float red[PARTP];
  __shared__ __attribute__((aligned(16))) u32 wph[164];
  __shared__ __attribute__((aligned(16))) u32x4 frag[8][64];
  const int tid = threadIdx.x, lane = tid & 63;
  const int w = __builtin_amdgcn_readfirstlane(tid >> 6);
  const int b = blockIdx.x;
  const int xcd = b & 7, j = b >> 3;
  const int head = xcd * 4 + (j & 3), rr = j >> 2;
  const int fr = lane & 15, fg = lane >> 4;
  STAMP2RT(30); STAMP2(1);

  const int pt = tid < PARTP / 4 ? tid : 0;
  const float4* psrc = (const float4*)(part + (size_t)head * PARTP) + pt;
  float4 pv[16];
#pragma unroll
  for (int r = 0; r < 16; ++r) pv[r] = psrc[(size_t)r * NH * PARTP / 4];
  const u32 wv = ((const u32*)(wphi + (size_t)head * DH * DH))[tid < 162 ? tid : 0];
  u32x4 qa[2], qb[2];
#pragma unroll
  for (int rt = 0; rt < 2; ++rt) {
    const int row = rr * 256 + w * 32 + rt * 16 + fr;
    const int fo = fg < 2 ? 8 * fg : 10;
    qa[rt] = *(const u32x4*)(qbuf + (size_t)row * QP + head * DH + fo);
    qb[rt] = *(const u32x4*)(qbuf + (size_t)row * QP + DM + head * DH + fo);
  }
#pragma unroll
  for (int rt = 0; rt < 2; ++rt) {
    if (fg == 2) { qa[rt] = u32x4{qa[rt][3], 0, 0, 0}; qb[rt] = u32x4{qb[rt][3], 0, 0, 0}; }
    if (fg == 3) { qa[rt] = u32x4{0, 0, 0, 0}; qb[rt] = u32x4{0, 0, 0, 0}; }
  }
  STAMP2(2);
  {
    float4 s = pv[0];
#pragma unroll
    for (int r = 1; r < 16; ++r) { s.x += pv[r].x; s.y += pv[r].y; s.z += pv[r].z; s.w += pv[r].w; }
    s.x = rbf(s.x); s.y = rbf(s.y); s.z = rbf(s.z); s.w = rbf(s.w);
    if (tid < PARTP / 4) *(float4*)(red + tid * 4) = s;
    if (tid < 162) wph[tid] = wv;
  }
  __syncthreads();
  STAMP2(3);
  {
    const int mt = (w < 2) ? w : ((w < 6) ? ((w - 2) >> 1) : (w - 6));
    float v[8];
    if (w < 6) {
      const bool isA = w < 2;
      const int okv = isA ? KVA_OFF : KVB_OFF, oz = isA ? ZA_OFF : ZB_OFF;
      const int ks = (w - 2) & 1;
#pragma unroll
      for (int jj = 0; jj < 8; ++jj) {
        int F; bool ok;
        if (isA) { F = 8 * fg + jj; ok = F < DH; }
        else if (ks == 0) { F = (jj < 4) ? 4 * fg + jj : 18 + 4 * fg + (jj - 4); ok = true; }
        else { F = (jj < 2) ? 16 + jj : 34 + (jj - 2); ok = (fg == 0) && (jj < 4); }
        const int Fc = ok ? F : 0;
        const int mcol = mt == 0 ? fr : 16 + (fr & 1);
        float a = red[okv + Fc * DH + mcol];
        const float zz = red[oz + Fc];
        if (mt == 1) a = fr < 2 ? a : (fr == 2 ? zz : 0.f);
        v[jj] = ok ? a : 0.f;
      }
    } else {
      const u16* wp = (const u16*)wph;
#pragma unroll
      for (int jj = 0; jj < 8; ++jj) {
        const int f = 8 * fg + jj;
        const int fc = f < DH ? f : 0;
        const u16 e = wp[fc * DH + (mt == 0 ? fr : 16 + (fr & 1))];
        const bool ok = f < DH && (mt == 0 || fr < 2);
        v[jj] = ok ? bf2f(e) : 0.f;
      }
    }
    frag[w][lane] = u32x4{pack2(v[0], v[1]), pack2(v[2], v[3]), pack2(v[4], v[5]), pack2(v[6], v[7])};
  }
  __syncthreads();
  STAMP2(4);
  const bf16x8 akA0 = __builtin_bit_cast(bf16x8, frag[0][lane]), akA1 = __builtin_bit_cast(bf16x8, frag[1][lane]);
  const bf16x8 akB00 = __builtin_bit_cast(bf16x8, frag[2][lane]), akB01 = __builtin_bit_cast(bf16x8, frag[3][lane]);
  const bf16x8 akB10 = __builtin_bit_cast(bf16x8, frag[4][lane]), akB11 = __builtin_bit_cast(bf16x8, frag[5][lane]);
  const bf16x8 wa0 = __builtin_bit_cast(bf16x8, frag[6][lane]), wa1 = __builtin_bit_cast(bf16x8, frag[7][lane]);
  STAMP2(5); STAMP2(6);

#pragma unroll
  for (int rt = 0; rt < 2; ++rt) {
    const int row = rr * 256 + w * 32 + rt * 16 + fr;
    {
      u32 pr[4];
#pragma unroll
      for (int jj = 0; jj < 4; ++jj) {
        const int f = 8 * fg + 2 * jj;
        float t0 = lo2f(qa[rt][jj]), t1 = hi2f(qa[rt][jj]);
        float v0 = (f < DH) ? phiA(t0) : 0.f, v1 = (f + 1 < DH) ? phiA(t1) : 0.f;
        pr[jj] = pack2(v0, v1);
      }
      bf16x8 pq = mk8(pr[0], pr[1], pr[2], pr[3]);
      f32x4 z4 = {0.f, 0.f, 0.f, 0.f};
      f32x4 d0 = MFMA16(akA0, pq, z4);
      f32x4 d1 = MFMA16(akA1, pq, z4);
      float den = xl_sum(fg == 0 ? d1[2] : 0.f);
      den = rbf(rbf(den) + 1e-6f);
      const float inv = __builtin_amdgcn_rcpf(den);
      u16* dst = obuf + (size_t)row * QP + head * DH;
      *(u32*)(dst + 4 * fg) = pack2(rbf(d0[0]) * inv, rbf(d0[1]) * inv);
      *(u32*)(dst + 4 * fg + 2) = pack2(rbf(d0[2]) * inv, rbf(d0[3]) * inv);
      if (fg == 0) *(u32*)(dst + 16) = pack2(rbf(d1[0]) * inv, rbf(d1[1]) * inv);
    }
    {
      HFeat f;
      hedgehog(__builtin_bit_cast(bf16x8, qb[rt]), wa0, wa1, fg, f);
      bf16x8 f0 = mk8(pack2(f.fp[0], f.fp[1]), pack2(f.fp[2], f.fp[3]), pack2(f.fm[0], f.fm[1]), pack2(f.fm[2], f.fm[3]));
      const u32 s0 = pack2(f.xp[0], f.xp[1]), s1 = pack2(f.xm[0], f.xm[1]);
      bf16x8 f1 = mk8(fg == 0 ? s0 : 0, fg == 0 ? s1 : 0, 0, 0);
      f32x4 z4 = {0.f, 0.f, 0.f, 0.f};
      f32x4 d0 = MFMA16(akB00, f0, z4); d0 = MFMA16(akB01, f1, d0);
      f32x4 d1 = MFMA16(akB10, f0, z4); d1 = MFMA16(akB11, f1, d1);
      float den = xl_sum(fg == 0 ? d1[2] : 0.f);
      den = rbf(rbf(den) + 1e-6f);
      const float inv = __builtin_amdgcn_rcpf(den);
      u16* dst = obuf + (size_t)row * QP + DM + head * DH;
      *(u32*)(dst + 4 * fg) = pack2(rbf(d0[0]) * inv, rbf(d0[1]) * inv);
      *(u32*)(dst + 4 * fg + 2) = pack2(rbf(d0[2]) * inv, rbf(d0[3]) * inv);
      if (fg == 0) *(u32*)(dst + 16) = pack2(rbf(d1[0]) * inv, rbf(d1[1]) * inv);
    }
  }
  STAMP2(7); STAMP2RT(31);
}

constexpr int K3_AB = 80 * 128;
constexpr int K3_BB = 64 * 128;
constexpr int K3_STG = K3_AB + K3_BB;
constexpr int K3_NBUF = 4;

__global__ __launch_bounds__(256, 1)
void k3_out(const u16* __restrict__ obuf, const u16* __restrict__ woA, const u16* __restrict__ woB,
            const float* __restrict__ gbuf, u16* __restrict__ out) {
  __shared__ __attribute__((aligned(16))) char smem[K3_NBUF * K3_STG];
  const int tid = threadIdx.x, lane = tid & 63;
  const int w = __builtin_amdgcn_readfirstlane(tid >> 6);
  const int b = blockIdx.x;
  const int L = (b & 7) * 30 + (b >> 3);
  if ((b >> 3) >= 30 || L >= 234) return;
  const int rbk = L / 9, cb = L % 9;
  const int fr = lane & 15, fg = lane >> 4;

  const char* asrc[3];
#pragma unroll
  for (int jj = 0; jj < 3; ++jj) {
    int piece = w + 4 * jj; if (piece > 9) piece = 9;
    const int rl = 8 * piece + (lane >> 3);
    int grow = rbk * 80 + rl; if (grow > SEQ - 1) grow = SEQ - 1;
    const int chunk = (lane & 7) ^ ((rl >> 1) & 7);
    asrc[jj] = (const char*)obuf + ((size_t)grow * QP + chunk * 8) * 2;
  }
  const char* bsrc[2];
#pragma unroll
  for (int jj = 0; jj < 2; ++jj) {
    const int kb = w + 4 * jj;
    const int t = lane >> 4, rpos = (lane & 15) >> 1, ch = lane & 1;
    const int kk = kb * 8 + (rpos ^ ((kb & 1) << 2));
    bsrc[jj] = (const char*)0 + ((size_t)kk * DM + cb * 64 + t * 16 + ch * 8) * 2;
  }
  const u32 lds0 = (u32)(uintptr_t)smem;
  auto issue = [&](int st) {
    const u32 sb = lds0 + (st % K3_NBUF) * K3_STG;
    const int brs = st / 9, ks = st % 9;
    const char* wbase = (const char*)(brs ? woB : woA);
#pragma unroll
    for (int jj = 0; jj < 3; ++jj)
      if (jj < 2 || w < 2) glds16(asrc[jj] + st * 128, __builtin_amdgcn_readfirstlane(sb + (w + 4 * jj) * 1024));
#pragma unroll
    for (int jj = 0; jj < 2; ++jj)
      glds16(wbase + (size_t)bsrc[jj] + (size_t)ks * 64 * DM * 2, __builtin_amdgcn_readfirstlane(sb + K3_AB + (w + 4 * jj) * 1024));
  };
  issue(0); issue(1); issue(2);
  float gv[5];
#pragma unroll
  for (int mt = 0; mt < 5; ++mt) { int grow = rbk * 80 + mt * 16 + fr; if (grow > SEQ - 1) grow = SEQ - 1; gv[mt] = gbuf[grow]; }

  const int aoff = fr * 128 + ((fg ^ ((fr >> 1) & 7)) << 4);
  int boff[2];
  {
    const int q = (lane & 15) >> 2, p = lane & 3;
#pragma unroll
    for (int hh = 0; hh < 2; ++hh)
      boff[hh] = K3_AB + (fg * 4 + w) * 256 + (((4 * hh + q) ^ ((fg & 1) << 2)) << 5) + (p << 3);
  }
  f32x4 acc[2][5];
#pragma unroll
  for (int i = 0; i < 2; ++i)
#pragma unroll
    for (int mt = 0; mt < 5; ++mt) acc[i][mt] = f32x4{0.f, 0.f, 0.f, 0.f};

#pragma unroll
  for (int st = 0; st < 18; ++st) {
    if (st < 16) { if (w < 2) WAIT_V(10); else WAIT_V(8); }
    else if (st == 16) { if (w < 2) WAIT_V(5); else WAIT_V(4); }
    else WAIT_V(0);
    WAIT_L0();
    RAW_BAR();
    if (st + 3 < 18) issue(st + 3);
    lds_cptr sb = (lds_cptr)smem + (st % K3_NBUF) * K3_STG;
#pragma unroll
    for (int s = 0; s < 2; ++s) {
      s16x4 lo = trread(sb + boff[0] + s * 4096);
      s16x4 hi = trread(sb + boff[1] + s * 4096);
      const bf16x8 wf = cat8(lo, hi);
#pragma unroll
      for (int mt = 0; mt < 5; ++mt) {
        const bf16x8 of = *(const AS3 bf16x8*)(sb + (aoff ^ (s * 64)) + mt * 2048);
        acc[st / 9][mt] = MFMA16(wf, of, acc[st / 9][mt]);
      }
    }
  }

#pragma unroll
  for (int mt = 0; mt < 5; ++mt) {
    const int grow = rbk * 80 + mt * 16 + fr;
    const float g = gv[mt];
    const float omg = rbf(1.0f - g);
    float r[4];
#pragma unroll
    for (int i = 0; i < 4; ++i) r[i] = rbf(rbf(g * rbf(acc[0][mt][i])) + rbf(omg * rbf(acc[1][mt][i])));
    if (grow < SEQ) {
      u32x2 ov = {pack2(r[0], r[1]), pack2(r[2], r[3])};
      *(u32x2*)(out + (size_t)grow * DM + cb * 64 + 16 * w + 4 * fg) = ov;
    }
  }
}

extern "C" void kernel_launch(void* const* d_in, const int* in_sizes, int n_in,
                              void* d_out, int out_size, void* d_ws, size_t ws_size,
                              hipStream_t stream) {
  const u16* x    = (const u16*)d_in[0];
  const u16* wA   = (const u16*)d_in[1];
  const u16* woA  = (const u16*)d_in[2];
  const u16* wB   = (const u16*)d_in[4];
  const u16* wphi = (const u16*)d_in[5];
  const u16* woB  = (const u16*)d_in[6];
  const u16* gw   = (const u16*)d_in[7];
  char* ws = (char*)d_ws;
  u16*   qbuf = (u16*)(ws);
  float* part = (float*)(ws + 4718592);
  u16*   obuf = (u16*)(ws + 6823936);
  float* gbuf = (float*)(ws + 11542528);
  k1_qkv<<<256, 512, 0, stream>>>(x, wA, wB, wphi, gw, qbuf, part, gbuf
  );
  k2_attn<<<256, 512, 0, stream>>>(qbuf, part, wphi, obuf
  );
  k3_out<<<256, 256, 0, stream>>>(obuf, woA, woB, gbuf, (u16*)d_out);
}
```

```cpp
#include <hip/hip_runtime.h>
#include <hip/hip_bf16.h>
#include <stdint.h>

typedef unsigned short u16;
typedef unsigned int u32;
typedef __attribute__((ext_vector_type(8))) short bf16x8;
typedef __attribute__((ext_vector_type(4))) short s16x4;
typedef __attribute__((ext_vector_type(4))) float f32x4;
typedef __attribute__((ext_vector_type(4))) unsigned u32x4;
typedef __attribute__((ext_vector_type(2))) unsigned u32x2;
typedef __attribute__((ext_vector_type(2))) __bf16 bf16x2n;
typedef __attribute__((address_space(3))) const char* lds_cptr;
typedef __attribute__((address_space(3))) char* lds_ptr;
typedef short v4i16_t __attribute__((ext_vector_type(4)));
#define AS3 __attribute__((address_space(3)))
#define DEV __device__ __forceinline__

constexpr int SEQ = 2048, DM = 576, NH = 32, DH = 18;
constexpr int QP = 1152;
constexpr int WP = 1728;
constexpr int PARTP = 1028;
constexpr int KVA_OFF = 0, ZA_OFF = 324, KVB_OFF = 344, ZB_OFF = 992;

DEV float bf2f(u16 h) { return __uint_as_float(((u32)h) << 16); }
DEV float rbf(float f) { return (float)(__bf16)f; }
DEV u32 pack2(float lo, float hi) { bf16x2n v = {(__bf16)lo, (__bf16)hi}; return __builtin_bit_cast(u32, v); }
DEV float lo2f(u32 p) { return __uint_as_float(p << 16); }
DEV float hi2f(u32 p) { return __uint_as_float(p & 0xffff0000u); }
DEV s16x4 trread(lds_cptr p) { return __builtin_bit_cast(s16x4, __builtin_amdgcn_ds_read_tr16_b64_v4i16((AS3 v4i16_t*)p)); }
DEV bf16x8 cat8(s16x4 lo, s16x4 hi) { return __builtin_shufflevector(lo, hi, 0, 1, 2, 3, 4, 5, 6, 7); }
DEV bf16x8 mk8(u32 a, u32 b, u32 c, u32 d) { u32x4 v = {a, b, c, d}; return __builtin_bit_cast(bf16x8, v); }
DEV void glds16(const void* g, u32 lds_base) {
  u32 sv;
  asm volatile("s_mov_b32 %0, m0\n\ts_mov_b32 m0, %2\n\ts_nop 0\n\tglobal_load_lds_dwordx4 %1, off\n\ts_mov_b32 m0, %0"
               : "=&s"(sv) : "v"(g), "s"(lds_base) : "memory");
}
#define MFMA16(a, b, c) __builtin_amdgcn_mfma_f32_16x16x32_bf16((a), (b), (c), 0, 0, 0)
#define WAIT_V(n) asm volatile("s_waitcnt vmcnt(" #n ")" ::: "memory")
#define WAIT_L0() asm volatile("s_waitcnt lgkmcnt(0)" ::: "memory")
#define RAW_BAR() __builtin_amdgcn_s_barrier()

DEV float xl_max(float v) {
  auto r = __builtin_amdgcn_permlane16_swap(__float_as_uint(v), __float_as_uint(v), false, false);
  v = fmaxf(__uint_as_float(r[0]), __uint_as_float(r[1]));
  auto s = __builtin_amdgcn_permlane32_swap(__float_as_uint(v), __float_as_uint(v), false, false);
  return fmaxf(__uint_as_float(s[0]), __uint_as_float(s[1]));
}
DEV float xl_min(float v) {
  auto r = __builtin_amdgcn_permlane16_swap(__float_as_uint(v), __float_as_uint(v), false, false);
  v = fminf(__uint_as_float(r[0]), __uint_as_float(r[1]));
  auto s = __builtin_amdgcn_permlane32_swap(__float_as_uint(v), __float_as_uint(v), false, false);
  return fminf(__uint_as_float(s[0]), __uint_as_float(s[1]));
}
DEV float xl_sum(float v) {
  auto r = __builtin_amdgcn_permlane16_swap(__float_as_uint(v), __float_as_uint(v), false, false);
  v = __uint_as_float(r[0]) + __uint_as_float(r[1]);
  auto s = __builtin_amdgcn_permlane32_swap(__float_as_uint(v), __float_as_uint(v), false, false);
  return __uint_as_float(s[0]) + __uint_as_float(s[1]);
}

DEV float phiA(float t) {
  float u = t > 0.f ? rbf(t + 1.0f) : rbf(__expf(t));
  return rbf(u + 1e-6f);
}

struct HFeat { float fp[4], fm[4], xp[2], xm[2]; };
DEV void hedgehog(bf16x8 raw, bf16x8 a0, bf16x8 a1, int g, HFeat& o) {
  f32x4 z4 = {0.f, 0.f, 0.f, 0.f};
  f32x4 d0 = MFMA16(a0, raw, z4);
  f32x4 d1 = MFMA16(a1, raw, z4);
  float p0 = rbf(d0[0]), p1 = rbf(d0[1]), p2 = rbf(d0[2]), p3 = rbf(d0[3]);
  float e0 = rbf(d1[0]), e1 = rbf(d1[1]);
  float mx = fmaxf(fmaxf(p0, p1), fmaxf(p2, p3));
  float mn = fminf(fminf(p0, p1), fminf(p2, p3));
  if (g == 0) { mx = fmaxf(mx, fmaxf(e0, e1)); mn = fminf(mn, fminf(e0, e1)); }
  mx = xl_max(mx);
  mn = xl_min(mn);
  float a[4] = {__expf(p0 - mx), __expf(p1 - mx), __expf(p2 - mx), __expf(p3 - mx)};
  float b[4] = {__expf(mn - p0), __expf(mn - p1), __expf(mn - p2), __expf(mn - p3)};
  float xa0 = __expf(e0 - mx), xa1 = __expf(e1 - mx), xb0 = __expf(mn - e0), xb1 = __expf(mn - e1);
  float sp = (a[0] + a[1]) + (a[2] + a[3]);
  float sm = (b[0] + b[1]) + (b[2] + b[3]);
  if (g == 0) { sp += xa0 + xa1; sm += xb0 + xb1; }
  sp = xl_sum(sp);
  sm = xl_sum(sm);
  float ip = 1.0f / sp, im = 1.0f / sm;
#pragma unroll
  for (int i = 0; i < 4; ++i) { o.fp[i] = rbf(a[i] * ip); o.fm[i] = rbf(b[i] * im); }
  o.xp[0] = rbf(xa0 * ip); o.xp[1] = rbf(xa1 * ip);
  o.xm[0] = rbf(xb0 * im); o.xm[1] = rbf(xb1 * im);
}

DEV void load_wphi_frags(const u16* __restrict__ wphi, int head, int lane, bf16x8& a0, bf16x8& a1) {
  const int m = lane & 15, g = lane >> 4;
  u32 w0[4], w1[4];
#pragma unroll
  for (int jj = 0; jj < 4; ++jj) {
    u32 v0 = 0, v1 = 0;
#pragma unroll
    for (int h2 = 0; h2 < 2; ++h2) {
      const int f = 8 * g + 2 * jj + h2;
      u32 e0 = 0, e1 = 0;
      if (f < DH) {
        e0 = wphi[(head * DH + f) * DH + m];
        if (m < 2) e1 = wphi[(head * DH + f) * DH + 16 + m];
      }
      v0 |= e0 << (16 * h2); v1 |= e1 << (16 * h2);
    }
    w0[jj] = v0; w1[jj] = v1;
  }
  a0 = mk8(w0[0], w0[1], w0[2], w0[3]);
  a1 = mk8(w1[0], w1[1], w1[2], w1[3]);
}

constexpr int K1_XB = 16384;
constexpr int K1_WPITCH = 448;
constexpr int K1_STAGE = K1_XB + 64 * K1_WPITCH;
constexpr int EP = 272;
constexpr int E_ROWS = 224;
constexpr int PHI_OFF = E_ROWS * EP;
constexpr int T2_OFF = PHI_OFF + 160 * EP;
constexpr int T2P = 208;
constexpr int K1_WPHI_OFF = 3 * K1_STAGE;
static_assert(T2_OFF + 128 * T2P <= 3 * K1_STAGE, "k1 lds");

__global__ __launch_bounds__(512, 1)
void k1_qkv(const u16* __restrict__ x, const u16* __restrict__ wA, const u16* __restrict__ wB,
            const u16* __restrict__ wphi, const u16* __restrict__ gate_w,
            u16* __restrict__ qbuf, float* __restrict__ part, float* __restrict__ gbuf
            ) {
  __shared__ __attribute__((aligned(16))) char smem[3 * K1_STAGE + 2624];
  const int tid = threadIdx.x, lane = tid & 63;
  const int w = __builtin_amdgcn_readfirstlane(tid >> 6);
  const int b = blockIdx.x;
  const int g4 = b & 7, br = (b >> 3) & 1, rb = b >> 4;
  const u16* __restrict__ W = br ? wB : wA;
  const int wm = w >> 1, wn = w & 1;
  const int fr = lane & 15, fg = lane >> 4;
  const u32 lds0 = (u32)(uintptr_t)smem;
  const char* xsrc[2];
#pragma unroll
  for (int j = 0; j < 2; ++j) {
    const int rl = (w * 2 + j) * 8 + (lane >> 3);
    const int chunk = (lane & 7) ^ ((rl >> 1) & 7);
    xsrc[j] = (const char*)x + (size_t)(rb * 128 + rl) * (DM * 2) + chunk * 16;
  }
  const int wi0 = (w < 4) ? 4 * w : 16 + 3 * (w - 4);
  const char* wsrc[4];
#pragma unroll
  for (int j = 0; j < 4; ++j) {
    int piece = wi0 + j; if (piece > 27) piece = 27;
    const int slot = piece * 64 + lane;
    const int k = slot / 28, pos = slot % 28;
    int u = pos ^ (((k >> 3) & 1) << 1);
    if (u > 26) u = 26;
    const int seg = u / 9, wi = u % 9;
    wsrc[j] = (const char*)W + (size_t)k * (WP * 2) + (seg * DM + g4 * 72 + wi * 8) * 2;
  }
  auto issue = [&](int st, int buf) {
    const u32 sb = lds0 + buf * K1_STAGE;
#pragma unroll
    for (int j = 0; j < 2; ++j)
      glds16(xsrc[j] + st * 128, __builtin_amdgcn_readfirstlane(sb + (w * 2 + j) * 1024));
#pragma unroll
    for (int j = 0; j < 3; ++j)
      glds16(wsrc[j] + (size_t)st * 64 * (WP * 2), __builtin_amdgcn_readfirstlane(sb + K1_XB + (wi0 + j) * 1024));
    if (w < 4) glds16(wsrc[3] + (size_t)st * 64 * (WP * 2), __builtin_amdgcn_readfirstlane(sb + K1_XB + (wi0 + 3) * 1024));
  };
  issue(0, 0); issue(1, 1);

  u16 gx[9], gwv[9];
  {
    const int row = b * 8 + w;
#pragma unroll
    for (int i = 0; i < 9; ++i) { gx[i] = x[(size_t)row * DM + i * 64 + lane]; gwv[i] = gate_w[i * 64 + lane]; }
  }

  if (br == 1) {
    const u32* src = (const u32*)(wphi + (size_t)g4 * 4 * DH * DH);
    for (int i = tid; i < 648; i += 512) *(u32*)(smem + K1_WPHI_OFF + i * 4) = src[i];
  }

  const int aoff = (wm * 32 + fr) * 128 + ((fg ^ ((fr >> 1) & 7)) << 4);
  int boff[7];
  {
    const int q = (lane & 15) >> 2, p = lane & 3;
#pragma unroll
    for (int nt = 0; nt < 7; ++nt) {
      const int t = wn * 7 + nt;
      boff[nt] = K1_XB + (8 * fg + q) * K1_WPITCH + ((2 * (t ^ (fg & 1)) + (p >> 1)) << 4) + ((p & 1) << 3);
    }
  }

  f32x4 acc[2][7];
#pragma unroll
  for (int mt = 0; mt < 2; ++mt)
#pragma unroll
    for (int nt = 0; nt < 7; ++nt) acc[mt][nt] = f32x4{0.f, 0.f, 0.f, 0.f};

#pragma unroll
  for (int it = 0; it < 9; ++it) {
    const int buf = it % 3;
    if (it < 8) { if (w < 4) WAIT_V(6); else WAIT_V(5); } else WAIT_V(0);
    WAIT_L0();
    RAW_BAR();
    if (it + 2 < 9) issue(it + 2, (it + 2) % 3);
    lds_cptr sb = (lds_cptr)smem + buf * K1_STAGE;
#pragma unroll
    for (int s = 0; s < 2; ++s) {
      bf16x8 a[2], bb[7];
#pragma unroll
      for (int mt = 0; mt < 2; ++mt) a[mt] = *(const AS3 bf16x8*)(sb + (aoff ^ (s * 64)) + mt * 2048);
#pragma unroll
      for (int nt = 0; nt < 7; ++nt) {
        s16x4 lo = trread(sb + boff[nt] + s * (32 * K1_WPITCH));
        s16x4 hi = trread(sb + boff[nt] + s * (32 * K1_WPITCH) + 4 * K1_WPITCH);
        bb[nt] = cat8(lo, hi);
      }
#pragma unroll
      for (int mt = 0; mt < 2; ++mt)
#pragma unroll
        for (int nt = 0; nt < 7; ++nt) acc[mt][nt] = MFMA16(a[mt], bb[nt], acc[mt][nt]);
    }
  }
  {
    float sg = 0.f;
#pragma unroll
    for (int i = 0; i < 9; ++i) sg += rbf(bf2f(gx[i]) * bf2f(gwv[i]));
    sg = xl_sum(sg);
    sg += __builtin_amdgcn_update_dpp(0.f, sg, 0x128, 0xf, 0xf, true);
    sg += __builtin_amdgcn_update_dpp(0.f, sg, 0x124, 0xf, 0xf, true);
    sg += __builtin_amdgcn_update_dpp(0.f, sg, 0x122, 0xf, 0xf, true);
    sg += __builtin_amdgcn_update_dpp(0.f, sg, 0x121, 0xf, 0xf, true);
    sg = rbf(sg);
    const float g = rbf(1.0f / (1.0f + __expf(-sg)));
    if (lane == 0) gbuf[b * 8 + w] = g;
  }

#pragma unroll
  for (int mt = 0; mt < 2; ++mt)
#pragma unroll
    for (int nt = 0; nt < 7; ++nt)
#pragma unroll
      for (int i = 0; i < 4; ++i) acc[mt][nt][i] = rbf(acc[mt][nt][i]);
  if (br == 0) {
    if (wn == 0) {
#pragma unroll
      for (int mt = 0; mt < 2; ++mt)
#pragma unroll
        for (int i = 0; i < 4; ++i) {
          acc[mt][4][i] = (fr >= 8) ? phiA(acc[mt][4][i]) : acc[mt][4][i];
          acc[mt][5][i] = phiA(acc[mt][5][i]);
          acc[mt][6][i] = phiA(acc[mt][6][i]);
        }
    } else {
#pragma unroll
      for (int mt = 0; mt < 2; ++mt)
#pragma unroll
        for (int i = 0; i < 4; ++i) {
          acc[mt][0][i] = phiA(acc[mt][0][i]);
          acc[mt][1][i] = phiA(acc[mt][1][i]);
        }
    }
  }
  __syncthreads();
  char* E = smem;
#pragma unroll
  for (int mt = 0; mt < 2; ++mt)
#pragma unroll
    for (int nt = 0; nt < 7; ++nt) {
      const int col = (wn * 7 + nt) * 16 + fr;
      const int row0 = wm * 32 + mt * 16 + fg * 4;
      u32x2 pk = {pack2(acc[mt][nt][0], acc[mt][nt][1]), pack2(acc[mt][nt][2], acc[mt][nt][3])};
      *(u32x2*)(E + col * EP + row0 * 2) = pk;
    }
  __syncthreads();
  lds_cptr Ec = (lds_cptr)smem;
  const int q4 = (lane & 15) >> 2, p4 = lane & 3;
  {
#pragma unroll
    for (int c = 0; c < 3; ++c) {
      int ch = 4 * c + fg;
      const bool valid = ch < 9;
      if (!valid) ch = 8;
      const int R0 = w * 16;
      s16x4 lo = trread(Ec + (8 * ch + q4) * EP + (R0 + 4 * p4) * 2);
      s16x4 hi = trread(Ec + (8 * ch + 4 + q4) * EP + (R0 + 4 * p4) * 2);
      if (valid) {
        u16* dst = qbuf + (size_t)(rb * 128 + R0 + fr) * QP + br * DM + g4 * 72 + 8 * ch;
        *(bf16x8*)dst = cat8(lo, hi);
      }
    }
  }
  const int hh = w >> 1, half = w & 1;
  const int head = g4 * 4 + hh;
  if (br == 1) {
    bf16x8 a0, a1;
    {
      const u16* wp = (const u16*)(smem + K1_WPHI_OFF) + hh * DH * DH;
      u32 w0[4], w1[4];
#pragma unroll
      for (int jj = 0; jj < 4; ++jj) {
        u32 v0 = 0, v1 = 0;
#pragma unroll
        for (int h2 = 0; h2 < 2; ++h2) {
          const int f = 8 * fg + 2 * jj + h2;
          const int fc = f < DH ? f : 0;
          u32 e0 = wp[fc * DH + fr], e1 = wp[fc * DH + 16 + (fr & 1)];
          e0 = f < DH ? e0 : 0; e1 = (f < DH && fr < 2) ? e1 : 0;
          v0 |= e0 << (16 * h2); v1 |= e1 << (16 * h2);
        }
        w0[jj] = v0; w1[jj] = v1;
      }
      a0 = mk8(w0[0], w0[1], w0[2], w0[3]); a1 = mk8(w1[0], w1[1], w1[2], w1[3]);
    }
    char* T2 = smem + T2_OFF;
#pragma unroll
    for (int r4 = 0; r4 < 4; ++r4) {
      const int R0 = (half * 4 + r4) * 16;
      const int c0 = 72 + 18 * hh + 8 * fg;
      s16x4 lo = trread(Ec + (c0 + q4) * EP + (R0 + 4 * p4) * 2);
      s16x4 hi = trread(Ec + (c0 + 4 + q4) * EP + (R0 + 4 * p4) * 2);
      const bf16x8 kb = cat8(lo, hi);
      f32x4 z4 = {0.f, 0.f, 0.f, 0.f};
      f32x4 d0 = MFMA16(a0, kb, z4);
      f32x4 d1 = MFMA16(a1, kb, z4);
      const int tok = R0 + fr;
      u32x2 pk = {pack2(rbf(d0[0]), rbf(d0[1])), pack2(rbf(d0[2]), rbf(d0[3]))};
      *(u32x2*)(T2 + tok * T2P + hh * 48 + fg * 8) = pk;
      if (fg == 0) *(u32*)(T2 + tok * T2P + hh * 48 + 32) = pack2(rbf(d1[0]), rbf(d1[1]));
    }
    __syncthreads();
    {
      const int tok = tid & 127, h4 = tid >> 7;
      const u32* src = (const u32*)(smem + T2_OFF + tok * T2P + h4 * 48);
      u32x4 v0 = *(const u32x4*)src, v1 = *(const u32x4*)(src + 4);
      u32 v2 = src[8];
      float p[18];
#pragma unroll
      for (int i = 0; i < 4; ++i) { p[2 * i] = lo2f(v0[i]); p[2 * i + 1] = hi2f(v0[i]); p[8 + 2 * i] = lo2f(v1[i]); p[8 + 2 * i + 1] = hi2f(v1[i]); }
      p[16] = lo2f(v2); p[17] = hi2f(v2);
      float mx = p[0], mn = p[0];
#pragma unroll
      for (int i = 1; i < 18; ++i) { mx = fmaxf(mx, p[i]); mn = fminf(mn, p[i]); }
      float ep[18], em[18], sp = 0.f, sm = 0.f;
#pragma unroll
      for (int i = 0; i < 18; ++i) { ep[i] = __expf(p[i] - mx); em[i] = __expf(mn - p[i]); sp += ep[i]; sm += em[i]; }
      const float ip = 1.0f / sp, im = 1.0f / sm;
      char* dst = smem + PHI_OFF + (h4 * 36) * EP + tok * 2;
#pragma unroll
      for (int i = 0; i < 18; ++i) {
        *(u16*)(dst + i * EP) = (u16)(pack2(rbf(ep[i] * ip), 0.f) & 0xffff);
        *(u16*)(dst + (18 + i) * EP) = (u16)(pack2(rbf(em[i] * im), 0.f) & 0xffff);
      }
    }
    __syncthreads();
  }
  {
    f32x4 kv0[2], kv1[2];
#pragma unroll
    for (int i = 0; i < 2; ++i) { kv0[i] = f32x4{0.f, 0.f, 0.f, 0.f}; kv1[i] = f32x4{0.f, 0.f, 0.f, 0.f}; }
    int vcol0 = 144 + 18 * hh + fr; int vcol1 = vcol0 + 16; if (vcol1 > E_ROWS - 1) vcol1 = E_ROWS - 1;
    const int abase = br ? (PHI_OFF + (hh * 36 + fr) * EP) : ((72 + 18 * hh + fr) * EP);
    const bool two = (br == 1) && (half == 0);
    const u32 one2 = 0x3f803f80u;
#pragma unroll
    for (int ks = 0; ks < 4; ++ks) {
      const int toff = (32 * ks + 8 * fg) * 2;
      bf16x8 vb0 = *(const AS3 bf16x8*)(Ec + vcol0 * EP + toff);
      bf16x8 vb1 = *(const AS3 bf16x8*)(Ec + vcol1 * EP + toff);
      if (fr == 2) vb1 = mk8(one2, one2, one2, one2);
      bf16x8 af = *(const AS3 bf16x8*)(Ec + abase + half * 16 * EP + toff);
      kv0[0] = MFMA16(af, vb0, kv0[0]);
      kv0[1] = MFMA16(af, vb1, kv0[1]);
      if (two) {
        bf16x8 af2 = *(const AS3 bf16x8*)(Ec + abase + 32 * EP + toff);
        kv1[0] = MFMA16(af2, vb0, kv1[0]);
        kv1[1] = MFMA16(af2, vb1, kv1[1]);
      }
    }
    float* dst = part + (size_t)(rb * NH + head) * PARTP;
    const int F = br ? 36 : 18;
    const int kvo = br ? KVB_OFF : KVA_OFF, zo = br ? ZB_OFF : ZA_OFF;
#pragma unroll
    for (int i = 0; i < 4; ++i) {
      const int m = 16 * half + 4 * fg + i;
      if (m < F) {
        dst[kvo + m * DH + fr] = kv0[0][i];
        if (fr < 2) dst[kvo + m * DH + 16 + fr] = kv0[1][i];
        if (fr == 2) dst[zo + m] = kv0[1][i];
      }
    }
    if (two) {
#pragma unroll
      for (int i = 0; i < 4; ++i) {
        const int m = 32 + 4 * fg + i;
        if (m < F) {
          dst[kvo + m * DH + fr] = kv1[0][i];
          if (fr < 2) dst[kvo + m * DH + 16 + fr] = kv1[1][i];
          if (fr == 2) dst[zo + m] = kv1[1][i];
        }
      }
    }
  }
  }

__global__ __launch_bounds__(512)
void k2_attn(const u16* __restrict__ qbuf, const float* __restrict__ part, const u16* __restrict__ wphi,
             u16* __restrict__ obuf
             ) {
  __shared__ __attribute__((aligned(16))) float red[PARTP];
  __shared__ __attribute__((aligned(16))) u32 wph[164];
  __shared__ __attribute__((aligned(16))) u32x4 frag[8][64];
  const int tid = threadIdx.x, lane = tid & 63;
  const int w = __builtin_amdgcn_readfirstlane(tid >> 6);
  const int b = blockIdx.x;
  const int xcd = b & 7, j = b >> 3;
  const int head = xcd * 4 + (j & 3), rr = j >> 2;
  const int fr = lane & 15, fg = lane >> 4;
  const int pt = tid < PARTP / 4 ? tid : 0;
  const float4* psrc = (const float4*)(part + (size_t)head * PARTP) + pt;
  float4 pv[16];
#pragma unroll
  for (int r = 0; r < 16; ++r) pv[r] = psrc[(size_t)r * NH * PARTP / 4];
  const u32 wv = ((const u32*)(wphi + (size_t)head * DH * DH))[tid < 162 ? tid : 0];
  u32x4 qa[1], qb[1];
#pragma unroll
  for (int rt = 0; rt < 1; ++rt) {
    const int row = rr * 128 + w * 16 + fr;
    const int fo = fg < 2 ? 8 * fg : 10;
    qa[rt] = *(const u32x4*)(qbuf + (size_t)row * QP + head * DH + fo);
    qb[rt] = *(const u32x4*)(qbuf + (size_t)row * QP + DM + head * DH + fo);
  }
#pragma unroll
  for (int rt = 0; rt < 1; ++rt) {
    if (fg == 2) { qa[rt] = u32x4{qa[rt][3], 0, 0, 0}; qb[rt] = u32x4{qb[rt][3], 0, 0, 0}; }
    if (fg == 3) { qa[rt] = u32x4{0, 0, 0, 0}; qb[rt] = u32x4{0, 0, 0, 0}; }
  }
  {
    float4 s = pv[0];
#pragma unroll
    for (int r = 1; r < 16; ++r) { s.x += pv[r].x; s.y += pv[r].y; s.z += pv[r].z; s.w += pv[r].w; }
    s.x = rbf(s.x); s.y = rbf(s.y); s.z = rbf(s.z); s.w = rbf(s.w);
    if (tid < PARTP / 4) *(float4*)(red + tid * 4) = s;
    if (tid < 162) wph[tid] = wv;
  }
  __syncthreads();
  {
    const int mt = (w < 2) ? w : ((w < 6) ? ((w - 2) >> 1) : (w - 6));
    float v[8];
    if (w < 6) {
      const bool isA = w < 2;
      const int okv = isA ? KVA_OFF : KVB_OFF, oz = isA ? ZA_OFF : ZB_OFF;
      const int ks = (w - 2) & 1;
#pragma unroll
      for (int jj = 0; jj < 8; ++jj) {
        int F; bool ok;
        if (isA) { F = 8 * fg + jj; ok = F < DH; }
        else if (ks == 0) { F = (jj < 4) ? 4 * fg + jj : 18 + 4 * fg + (jj - 4); ok = true; }
        else { F = (jj < 2) ? 16 + jj : 34 + (jj - 2); ok = (fg == 0) && (jj < 4); }
        const int Fc = ok ? F : 0;
        const int mcol = mt == 0 ? fr : 16 + (fr & 1);
        float a = red[okv + Fc * DH + mcol];
        const float zz = red[oz + Fc];
        if (mt == 1) a = fr < 2 ? a : (fr == 2 ? zz : 0.f);
        v[jj] = ok ? a : 0.f;
      }
    } else {
      const u16* wp = (const u16*)wph;
#pragma unroll
      for (int jj = 0; jj < 8; ++jj) {
        const int f = 8 * fg + jj;
        const int fc = f < DH ? f : 0;
        const u16 e = wp[fc * DH + (mt == 0 ? fr : 16 + (fr & 1))];
        const bool ok = f < DH && (mt == 0 || fr < 2);
        v[jj] = ok ? bf2f(e) : 0.f;
      }
    }
    frag[w][lane] = u32x4{pack2(v[0], v[1]), pack2(v[2], v[3]), pack2(v[4], v[5]), pack2(v[6], v[7])};
  }
  __syncthreads();
  const bf16x8 akA0 = __builtin_bit_cast(bf16x8, frag[0][lane]), akA1 = __builtin_bit_cast(bf16x8, frag[1][lane]);
  const bf16x8 akB00 = __builtin_bit_cast(bf16x8, frag[2][lane]), akB01 = __builtin_bit_cast(bf16x8, frag[3][lane]);
  const bf16x8 akB10 = __builtin_bit_cast(bf16x8, frag[4][lane]), akB11 = __builtin_bit_cast(bf16x8, frag[5][lane]);
  const bf16x8 wa0 = __builtin_bit_cast(bf16x8, frag[6][lane]), wa1 = __builtin_bit_cast(bf16x8, frag[7][lane]);
  #pragma unroll
  for (int rt = 0; rt < 1; ++rt) {
    const int row = rr * 128 + w * 16 + fr;
    {
      u32 pr[4];
#pragma unroll
      for (int jj = 0; jj < 4; ++jj) {
        const int f = 8 * fg + 2 * jj;
        float t0 = lo2f(qa[rt][jj]), t1 = hi2f(qa[rt][jj]);
        float v0 = (f < DH) ? phiA(t0) : 0.f, v1 = (f + 1 < DH) ? phiA(t1) : 0.f;
        pr[jj] = pack2(v0, v1);
      }
      bf16x8 pq = mk8(pr[0], pr[1], pr[2], pr[3]);
      f32x4 z4 = {0.f, 0.f, 0.f, 0.f};
      f32x4 d0 = MFMA16(akA0, pq, z4);
      f32x4 d1 = MFMA16(akA1, pq, z4);
      float den = xl_sum(fg == 0 ? d1[2] : 0.f);
      den = rbf(rbf(den) + 1e-6f);
      const float inv = __builtin_amdgcn_rcpf(den);
      u16* dst = obuf + (size_t)row * QP + head * DH;
      *(u32*)(dst + 4 * fg) = pack2(rbf(d0[0]) * inv, rbf(d0[1]) * inv);
      *(u32*)(dst + 4 * fg + 2) = pack2(rbf(d0[2]) * inv, rbf(d0[3]) * inv);
      if (fg == 0) *(u32*)(dst + 16) = pack2(rbf(d1[0]) * inv, rbf(d1[1]) * inv);
    }
    {
      HFeat f;
      hedgehog(__builtin_bit_cast(bf16x8, qb[rt]), wa0, wa1, fg, f);
      bf16x8 f0 = mk8(pack2(f.fp[0], f.fp[1]), pack2(f.fp[2], f.fp[3]), pack2(f.fm[0], f.fm[1]), pack2(f.fm[2], f.fm[3]));
      const u32 s0 = pack2(f.xp[0], f.xp[1]), s1 = pack2(f.xm[0], f.xm[1]);
      bf16x8 f1 = mk8(fg == 0 ? s0 : 0, fg == 0 ? s1 : 0, 0, 0);
      f32x4 z4 = {0.f, 0.f, 0.f, 0.f};
      f32x4 d0 = MFMA16(akB00, f0, z4); d0 = MFMA16(akB01, f1, d0);
      f32x4 d1 = MFMA16(akB10, f0, z4); d1 = MFMA16(akB11, f1, d1);
      float den = xl_sum(fg == 0 ? d1[2] : 0.f);
      den = rbf(rbf(den) + 1e-6f);
      const float inv = __builtin_amdgcn_rcpf(den);
      u16* dst = obuf + (size_t)row * QP + DM + head * DH;
      *(u32*)(dst + 4 * fg) = pack2(rbf(d0[0]) * inv, rbf(d0[1]) * inv);
      *(u32*)(dst + 4 * fg + 2) = pack2(rbf(d0[2]) * inv, rbf(d0[3]) * inv);
      if (fg == 0) *(u32*)(dst + 16) = pack2(rbf(d1[0]) * inv, rbf(d1[1]) * inv);
    }
  }
  }

constexpr int K3_AB = 2 * 80 * 128;
constexpr int K3_BB = 128 * 128;
constexpr int K3_STG = K3_AB + K3_BB;
constexpr int K3_NBUF = 4;

__global__ __launch_bounds__(512, 1)
void k3_out(const u16* __restrict__ obuf, const u16* __restrict__ woA, const u16* __restrict__ woB,
            const float* __restrict__ gbuf, u16* __restrict__ out
            ) {
  __shared__ __attribute__((aligned(16))) char smem[K3_NBUF * K3_STG];
  const int tid = threadIdx.x, lane = tid & 63;
  const int w = __builtin_amdgcn_readfirstlane(tid >> 6);
  const int b = blockIdx.x;
  const int L = (b & 7) * 30 + (b >> 3);
  if ((b >> 3) >= 30 || L >= 234) return;
  const int rbk = L / 9, cb = L % 9;
  const int fr = lane & 15, fg = lane >> 4;
  const int wn = w & 3, wm = w >> 2;
  u32 soff[5]; u32 doff[5];
#pragma unroll
  for (int jj = 0; jj < 5; ++jj) {
    int piece = w + 8 * jj; if (piece > 35) piece = 35;
    if (piece < 20) {
      const int h = piece / 10, rl = 8 * (piece % 10) + (lane >> 3);
      int grow = rbk * 80 + rl; if (grow > SEQ - 1) grow = SEQ - 1;
      const int chunk = (lane & 7) ^ ((rl >> 1) & 7);
      soff[jj] = ((u32)grow * QP + h * 64 + chunk * 8) * 2;
      doff[jj] = piece * 1024;
    } else {
      const int kb = piece - 20;
      const int t = lane >> 4, rpos = (lane & 15) >> 1, ch = lane & 1;
      const int kk = kb * 8 + (rpos ^ ((kb & 1) << 2));
      soff[jj] = ((u32)kk * DM + cb * 64 + t * 16 + ch * 8) * 2;
      doff[jj] = K3_AB + kb * 1024;
    }
  }
  const u32 lds0 = (u32)(uintptr_t)smem;
  auto issue = [&](int st) {
    const u32 sb = lds0 + (st % K3_NBUF) * K3_STG;
#pragma unroll
    for (int jj = 0; jj < 5; ++jj) {
      if (jj == 4 && w >= 4) break;
      const int piece = w + 8 * jj;
      const char* src;
      if (piece < 20) src = (const char*)obuf + soff[jj] + st * 256;
      else {
        const int kb = piece - 20;
        const int kglob0 = st * 128 + kb * 8;
        const bool isB = kglob0 >= DM;
        src = (const char*)(isB ? woB : woA) + soff[jj] + (size_t)(st * 128 - (isB ? DM : 0)) * (DM * 2);
      }
      glds16(src, __builtin_amdgcn_readfirstlane(sb + doff[jj]));
    }
  };
  issue(0); issue(1); issue(2);
  float gv[3];
#pragma unroll
  for (int i = 0; i < 3; ++i) { int grow = rbk * 80 + (wm * 3 + i) * 16 + fr; if (grow > SEQ - 1) grow = SEQ - 1; gv[i] = gbuf[grow]; }

  const int aoff = (wm * 48 + fr) * 128 + ((fg ^ ((fr >> 1) & 7)) << 4);
  int boff[2];
  {
    const int q = (lane & 15) >> 2, p = lane & 3;
#pragma unroll
    for (int hh = 0; hh < 2; ++hh)
      boff[hh] = K3_AB + (fg * 4 + wn) * 256 + (((4 * hh + q) ^ ((fg & 1) << 2)) << 5) + (p << 3);
  }
  f32x4 acc[2][3];
#pragma unroll
  for (int i = 0; i < 2; ++i)
#pragma unroll
    for (int mt = 0; mt < 3; ++mt) acc[i][mt] = f32x4{0.f, 0.f, 0.f, 0.f};

#pragma unroll
  for (int st = 0; st < 9; ++st) {
    if (st < 7) { if (w < 4) WAIT_V(10); else WAIT_V(8); }
    else if (st == 7) { if (w < 4) WAIT_V(5); else WAIT_V(4); }
    else WAIT_V(0);
    WAIT_L0();
    RAW_BAR();
    if (st + 3 < 9) issue(st + 3);
    lds_cptr sb = (lds_cptr)smem + (st % K3_NBUF) * K3_STG;
#pragma unroll
    for (int s = 0; s < 4; ++s) {
      const int kg = st * 128 + s * 32;
      s16x4 lo = trread(sb + boff[0] + s * 4096);
      s16x4 hi = trread(sb + boff[1] + s * 4096);
      const bf16x8 wf = cat8(lo, hi);
#pragma unroll
      for (int mt = 0; mt < 3; ++mt) {
        if (mt == 2 && wm == 1) continue;
        const bf16x8 of = *(const AS3 bf16x8*)(sb + (aoff ^ ((s & 1) * 64)) + (s >> 1) * 10240 + mt * 2048);
        acc[kg / DM][mt] = MFMA16(wf, of, acc[kg / DM][mt]);
      }
    }
  }

#pragma unroll
  for (int mt = 0; mt < 3; ++mt) {
    if (mt == 2 && wm == 1) continue;
    const int grow = rbk * 80 + (wm * 3 + mt) * 16 + fr;
    const float g = gv[mt];
    const float omg = rbf(1.0f - g);
    float r[4];
#pragma unroll
    for (int i = 0; i < 4; ++i) r[i] = rbf(rbf(g * rbf(acc[0][mt][i])) + rbf(omg * rbf(acc[1][mt][i])));
    if (grow < SEQ) {
      u32x2 ov = {pack2(r[0], r[1]), pack2(r[2], r[3])};
      *(u32x2*)(out + (size_t)grow * DM + cb * 64 + 16 * wn + 4 * fg) = ov;
    }
  }
  }

extern "C" void kernel_launch(void* const* d_in, const int* in_sizes, int n_in,
                              void* d_out, int out_size, void* d_ws, size_t ws_size,
                              hipStream_t stream) {
  const u16* x    = (const u16*)d_in[0];
  const u16* wA   = (const u16*)d_in[1];
  const u16* woA  = (const u16*)d_in[2];
  const u16* wB   = (const u16*)d_in[4];
  const u16* wphi = (const u16*)d_in[5];
  const u16* woB  = (const u16*)d_in[6];
  const u16* gw   = (const u16*)d_in[7];
  char* ws = (char*)d_ws;
  u16*   qbuf = (u16*)(ws);
  float* part = (float*)(ws + 4718592);
  u16*   obuf = (u16*)(ws + 6823936);
  float* gbuf = (float*)(ws + 11542528);
  k1_qkv<<<256, 512, 0, stream>>>(x, wA, wB, wphi, gw, qbuf, part, gbuf
  );
  k2_attn<<<512, 512, 0, stream>>>(qbuf, part, wphi, obuf
  );
  k3_out<<<256, 512, 0, stream>>>(obuf, woA, woB, gbuf, (u16*)d_out
  );
}
```
